```python
import torch

def _softmax(x, axis=-1):
    return torch.softmax(x, dim=axis)
def _gelu(x):
    return torch.nn.functional.gelu(x, approximate="tanh")
def _silu(x):
    return torch.nn.functional.silu(x)
def _sigmoid(x):
    return torch.sigmoid(x)
def _layernorm(x, axis=-1, eps=1e-5):
    m = x.mean(dim=axis, keepdim=True)
    v = x.var(dim=axis, keepdim=True, unbiased=False)
    return (x - m) / torch.sqrt(v + eps)
def _scatter_add(tgt, idx, src, axis=0):
    return tgt.clone().index_add_(axis, idx, src)
def _segment_sum(src, seg, n):
    out = torch.zeros((n,) + src.shape[1:], dtype=src.dtype)
    return out.index_add_(0, seg, src)
def _pad(x, pw):
    flat = tuple(v for pair in reversed(pw) for v in pair)
    return torch.nn.functional.pad(x, flat)

kernel_name = 'concat_heads__softmax_full_mha__spectral_gfnet__prompt'

def setup_inputs():

    _w = (lambda a: (lambda t: t.to(torch.bfloat16) if t.is_floating_point() else t)(torch.as_tensor(a)))
    def _A_setup():
        B, S, D = 1, 2048, 448
        return {
            "x": _w((lambda *s: torch.randn(*s, dtype=torch.bfloat16) * 0.2)(B, S, D)),
            "wqkv": _w((lambda *s: torch.randn(*s, dtype=torch.bfloat16) * 0.2)(D, 3 * D)),
            "wo": _w((lambda *s: torch.randn(*s, dtype=torch.bfloat16) * 0.05)(D, D)),
        }
    _A_in = _A_setup()

    def _B_setup():
        B, S, D = 1, 2048, 448
        Df = D // 2 + 1
        return {
            "x": _w((lambda *s: torch.randn(*s, dtype=torch.bfloat16) * 0.05)(B, S, D)),
            "w_r": _w((lambda *s: torch.randn(*s, dtype=torch.bfloat16) * 6.4)(S, Df) * 0.02),
            "w_i": _w((lambda *s: torch.randn(*s, dtype=torch.bfloat16) * 6.4)(S, Df) * 0.02),
        }
    _B_in = _B_setup()

    return {
        "A_x": _A_in["x"],
        "A_wqkv": _A_in["wqkv"],
        "A_wo": _A_in["wo"],
        "B_x": _B_in["x"],
        "B_w_r": _B_in["w_r"],
        "B_w_i": _B_in["w_i"],
    }

def reference(A_x, A_wqkv, A_wo, B_x, B_w_r, B_w_i):
    def _A_ref(x, wqkv, wo):
        B, S, D = x.shape
        H = 16; Dh = D // H
        qkv = (lambda x, *d: x.permute(*d))((x @ wqkv).reshape(B, S, 3, H, Dh), 2, 0, 3, 1, 4)
        q, k, v = qkv[0], qkv[1], qkv[2]
        scale = Dh ** -0.5
        scores = torch.einsum("bhqd,bhkd->bhqk", q, k) * scale
        p = _softmax(scores, axis=-1)
        o = torch.einsum("bhqk,bhkd->bhqd", p, v)
        return (lambda x, *d: x.permute(*d))(o, 0, 2, 1, 3).reshape(B, S, D) @ wo

    def _B_ref(x, w_r, w_i):
        B, S, D = x.shape
        X = (lambda x, axes=(-2,-1): torch.fft.rfft2(x.float(), dim=axes))(x, axes=(-2, -1))
        W = w_r + 1j * w_i
        X = X * W
        y = (lambda x, s, axes=(-2,-1): torch.fft.irfft2(x, s=s, dim=axes).to(torch.bfloat16))(X, (S, D), axes=(-2, -1))
        return y

    a = _A_ref(A_x, A_wqkv, A_wo)
    b = _B_ref(B_x, B_w_r, B_w_i)
    return torch.concatenate([a, b], axis=-1)
```

```cpp
#include <hip/hip_runtime.h>
#include <hip/hip_bf16.h>
#include <stdint.h>

#if defined(__HIP_DEVICE_COMPILE__)
namespace tb {
constexpr double PI = 3.14159265358979323846;
constexpr double ccos(double x) { double x2 = x * x, t = 1, s = 1; for (int i = 1; i < 18; ++i) { t *= -x2 / ((2 * i - 1) * (2 * i)); s += t; } return s; }
constexpr uint16_t bf(double d) { float f = (float)d; uint32_t u = __builtin_bit_cast(uint32_t, f); u += 0x7fffu + ((u >> 16) & 1u); return (uint16_t)(u >> 16); }
template <int N> struct CT { uint16_t c[N]; };
template <int N> constexpr CT<N> mk(double sc) { CT<N> r{}; for (int i = 0; i < N; ++i) { int j = i > N / 2 ? N - i : i; r.c[i] = (4 * j == N) ? 0 : bf(sc * ccos(2 * PI * j / N)); } return r; }
constexpr CT<448> g2 = mk<448>(2.0 / (2048.0 * 448.0)), g1 = mk<448>(1.0 / (2048.0 * 448.0));
struct GC { uint32_t w[16 * 256]; };
constexpr int kof(int jb, int par, int i) { int k0 = 29 * jb, k = k0 + ((k0 ^ par) & 1) + 2 * i; return (k < k0 + 29 && k <= 224) ? k : -1; }
constexpr uint32_t gval(int d, int k, int c) { if (k < 0) return 0; int a = (d * k + c * 112) % 448; return (k == 0 || k == 224) ? g1.c[a] : g2.c[a]; }
constexpr GC mkG(int dt) { GC r{}; int o = 0;
    for (int par = 0; par < 2; ++par) for (int jb = 0; jb < 8; ++jb) for (int l = 0; l < 64; ++l) { int d = 16 * dt + (l & 15), c = l >> 5, i0 = 4 * ((l >> 4) & 1);
        for (int j = 0; j < 4; ++j) { int i = i0 + 8 * (j >> 1) + 2 * (j & 1); r.w[o++] = gval(d, kof(jb, par, i), c) | gval(d, kof(jb, par, i + 1), c) << 16; } }
    return r; }
#define G_(i) constexpr GC g_##i = mkG(i);
G_(0) G_(1) G_(2) G_(3) G_(4) G_(5) G_(6) G_(7) G_(8) G_(9) G_(10) G_(11) G_(12) G_(13)
struct alignas(256) Tab { GC g[14]; };
}
static __device__ const tb::Tab dft_tab = {
    {tb::g_0, tb::g_1, tb::g_2, tb::g_3, tb::g_4, tb::g_5, tb::g_6, tb::g_7, tb::g_8, tb::g_9, tb::g_10, tb::g_11, tb::g_12, tb::g_13}};
#define TAB_PTR ((const void*)&dft_tab)
#else
#define TAB_PTR ((const void*)0)
#endif

__global__ __launch_bounds__(512, 2) void concat_heads__softmax_full_mha__spectral_gfnet__prompt_kernel(
    const void* xa, const void* wqkv, const void* wo, const void* xb, const void* wr, const void* wi, void* out, void* ws) {
    unsigned tid = threadIdx.x, blk = blockIdx.x;
#if defined(__HIP_DEVICE_COMPILE__)
    auto args = __builtin_amdgcn_kernarg_segment_ptr();
#else
    void* args = nullptr;
#endif
    asm volatile(
"L_beg_%=:\n"
"s_load_dwordx2 s[4:5], s[0:1], 0\n"
"s_load_dwordx2 s[8:9], s[0:1], 8\n"
"s_load_dwordx2 s[12:13], s[0:1], 56\n"
"s_load_dwordx2 s[16:17], s[0:1], 24\n"
"s_load_dwordx2 s[24:25], s[0:1], 16\n"
"s_load_dwordx2 s[28:29], s[0:1], 48\n"
"s_load_dwordx2 s[36:37], s[0:1], 32\n"
"s_load_dwordx2 s[40:41], s[0:1], 40\n"
"v_lshrrev_b32 v2, 6, v0\n"
"v_and_b32 v1, 63, v0\n"
"s_mov_b32 s44, s2\n"
"s_and_b32 s68, s2, 7\n"
"s_lshr_b32 s69, s2, 3\n"
"v_readfirstlane_b32 s45, v2\n"
"s_and_b32 s70, s68, 3\n"
"s_lshl_b32 s70, s70, 2\n"
"s_and_b32 s71, s69, 3\n"
"s_add_u32 s57, s70, s71\n"
"s_lshr_b32 s70, s68, 2\n"
"s_lshl_b32 s70, s70, 3\n"
"s_lshr_b32 s71, s69, 2\n"
"s_add_u32 s56, s70, s71\n"
"s_and_b32 s58, s57, 1\n"
"s_lshr_b32 s59, s57, 1\n"
"s_cmp_lt_u32 s57, 14\n"
"s_cselect_b32 s50, 1204224, 0\n"
"s_cselect_b32 s54, 10158080, 0\n"
"s_and_b32 s46, s45, 3\n"
"s_lshr_b32 s47, s45, 2\n"
"s_lshl_b32 s65, s45, 10\n"
"v_lshlrev_b32 v0, 4, v1\n"
"s_cmp_lg_u32 s45, 2\n"
"s_cbranch_scc1 L_pf_5_%=\n"
"s_getpc_b64 s[96:97]\n"
"L_pc_6_%=:\n"
"s_sub_u32 s96, s96, L_pc_6_%= - L_beg_%=\n"
"s_subb_u32 s97, s97, 0\n"
"s_mov_b32 s98, L_end_%= - L_beg_%=\n"
"s_mov_b32 s99, 0x20000\n"
"s_lshl_b32 s70, s69, 10\n"
"v_and_b32 v7, 7, v1\n"
"v_lshlrev_b32 v7, 7, v7\n"
"buffer_load_dword v224, v7, s[96:99], s70 offen\n"
"L_pf_5_%=:\n"
"s_cmp_lg_u32 s47, 0\n"
"s_cbranch_scc0 L_q_26_%=\n"
"L_b_1_%=:\n"
"v_lshrrev_b32 v2, 3, v1\n"
"v_lshrrev_b32 v3, 4, v1\n"
"v_and_b32 v6, 7, v1\n"
"s_and_b32 s70, s45, 1\n"
"s_lshl_b32 s70, s70, 2\n"
"s_lshl_b32 s74, s46, 10\n"
"v_or_b32 v7, s70, v3\n"
"v_xor_b32 v7, v7, v6\n"
"v_mul_u32_u24 v4, 896, v2\n"
"v_lshl_add_u32 v4, v7, 4, v4\n"
"s_mov_b32 s6, 1835008\n"
"s_mov_b32 s7, 0x20000\n"
"s_mul_i32 s76, s56, 114688\n"
"s_mul_i32 s70, s46, 7168\n"
"s_add_u32 s76, s76, s70\n"
"s_add_u32 s77, s76, 28672\n"
"v_and_b32 v7, 1, v3\n"
"v_lshlrev_b32 v7, 2, v7\n"
"v_xor_b32 v7, v7, v6\n"
"v_mul_u32_u24 v5, 2688, v2\n"
"v_lshl_add_u32 v5, v7, 4, v5\n"
"v_add_u32 v12, 86016, v5\n"
"s_mul_i32 s78, s58, 896\n"
"s_lshl_b32 s70, s59, 7\n"
"s_add_u32 s78, s78, s70\n"
"s_mul_i32 s70, s46, 21504\n"
"s_add_u32 s78, s78, s70\n"
"s_add_u32 s78, s78, 896\n"
"s_mov_b32 s51, 0x20000\n"
"s_waitcnt lgkmcnt(0)\n"
"s_mov_b32 s48, s8\n"
"s_mov_b32 s49, s9\n"
"s_add_u32 s1, s76, 57344\n"
"s_add_u32 s3, s76, 86016\n"
"s_add_u32 s70, s57, s56\n"
"s_and_b32 s70, s70, 1\n"
"s_mul_i32 s70, s70, 3\n"
"s_lshl_b32 s34, s70, 7\n"
"s_add_u32 m0, s74, 1024\n"
"s_add_u32 s35, s76, s34\n"
"buffer_load_dwordx4 v4, s[4:7], s35 offen lds\n"
"s_add_u32 m0, s74, 5120\n"
"s_add_u32 s35, s77, s34\n"
"buffer_load_dwordx4 v4, s[4:7], s35 offen lds\n"
"s_add_u32 m0, s74, 9216\n"
"s_add_u32 s35, s1, s34\n"
"buffer_load_dwordx4 v4, s[4:7], s35 offen lds\n"
"s_add_u32 m0, s74, 13312\n"
"s_add_u32 s35, s3, s34\n"
"buffer_load_dwordx4 v4, s[4:7], s35 offen lds\n"
"s_add_u32 m0, s74, 17408\n"
"s_mul_i32 s35, s34, 1344\n"
"s_add_u32 s35, s35, s78\n"
"buffer_load_dwordx4 v5, s[48:51], s35 offen lds\n"
"s_add_u32 m0, s74, 21504\n"
"s_nop 0\n"
"buffer_load_dwordx4 v12, s[48:51], s35 offen lds\n"
"s_add_u32 s34, s34, 128\n"
"s_cmp_eq_u32 s34, 896\n"
"s_cselect_b32 s34, 0, s34\n"
"s_mov_b32 s10, 1204224\n"
"s_mov_b32 s11, 0x20000\n"
"s_mov_b32 s18, 1835008\n"
"s_mov_b32 s19, 0x20000\n"
"s_mov_b32 s22, 229376\n"
"s_mov_b32 s23, 0x20000\n"
"s_mov_b32 s26, 401408\n"
"s_mov_b32 s27, 0x20000\n"
"s_mov_b32 s30, 3670016\n"
"s_mov_b32 s31, 0x20000\n"
"s_mov_b32 s14, 10158080\n"
"s_mov_b32 s15, 0x20000\n"
"buffer_load_dwordx2 v[254:255], off, s[12:15], 0 sc1\n"
"s_add_u32 m0, s74, 25600\n"
"s_add_u32 s35, s76, s34\n"
"buffer_load_dwordx4 v4, s[4:7], s35 offen lds\n"
"s_mov_b32 s38, 921600\n"
"s_mov_b32 s39, 0x20000\n"
"s_mov_b32 s42, 921600\n"
"s_mov_b32 s43, 0x20000\n"
"s_mov_b32 s52, s12\n"
"s_mov_b32 s53, s13\n"
"s_mov_b32 s55, 0x20000\n"
"s_mov_b32 s85, 200000\n"
"s_add_u32 m0, s74, 29696\n"
"s_add_u32 s35, s77, s34\n"
"buffer_load_dwordx4 v4, s[4:7], s35 offen lds\n"
"s_lshl_b32 s70, s68, 3\n"
"s_lshr_b32 s71, s69, 2\n"
"s_add_u32 s61, s70, s71\n"
"s_and_b32 s62, s69, 3\n"
"s_lshl_b32 s70, s68, 2\n"
"s_lshr_b32 s71, s69, 3\n"
"s_add_u32 s63, s70, s71\n"
"s_and_b32 s64, s69, 7\n"
"s_mul_i32 s60, s57, 7\n"
"s_lshr_b32 s60, s60, 1\n"
"s_lshl_b32 s79, s62, 7\n"
"s_min_u32 s79, s79, 320\n"
"s_lshl_b32 s83, s62, 2\n"
"s_min_u32 s83, s83, 10\n"
"s_add_u32 m0, s74, 33792\n"
"s_add_u32 s35, s1, s34\n"
"buffer_load_dwordx4 v4, s[4:7], s35 offen lds\n"
"v_lshrrev_b32 v20, 3, v1\n"
"s_lshl_b32 s70, s46, 3\n"
"v_add_u32 v20, s70, v20\n"
"v_bfe_u32 v21, v20, 1, 3\n"
"v_and_b32 v22, 7, v1\n"
"v_xor_b32 v21, v21, v22\n"
"s_add_u32 m0, s74, 37888\n"
"s_add_u32 s35, s3, s34\n"
"buffer_load_dwordx4 v4, s[4:7], s35 offen lds\n"
"v_mul_u32_u24 v13, 57344, v20\n"
"v_lshl_add_u32 v13, v21, 4, v13\n"
"v_lshrrev_b32 v20, 2, v1\n"
"v_mul_u32_u24 v14, 2688, v20\n"
"v_and_b32 v7, 3, v1\n"
"v_lshl_add_u32 v14, v7, 4, v14\n"
"s_add_u32 m0, s74, 41984\n"
"s_mul_i32 s35, s34, 1344\n"
"s_add_u32 s35, s35, s78\n"
"buffer_load_dwordx4 v5, s[48:51], s35 offen lds\n"
"v_and_b32 v2, 31, v1\n"
"v_lshrrev_b32 v3, 5, v1\n"
"s_mov_b32 s70, 123904\n"
"v_bfe_u32 v20, v2, 1, 3\n"
"v_lshl_add_u32 v21, v2, 7, s70\n"
"v_or_b32 v22, 0, v3\n"
"v_xor_b32 v22, v22, v20\n"
"v_lshl_add_u32 v8, v22, 4, v21\n"
"v_or_b32 v22, 2, v3\n"
"v_xor_b32 v22, v22, v20\n"
"v_lshl_add_u32 v9, v22, 4, v21\n"
"s_add_u32 m0, s74, 46080\n"
"s_nop 0\n"
"buffer_load_dwordx4 v12, s[48:51], s35 offen lds\n"
"s_add_u32 s34, s34, 128\n"
"s_cmp_eq_u32 s34, 896\n"
"s_cselect_b32 s34, 0, s34\n"
"v_or_b32 v22, 4, v3\n"
"v_xor_b32 v22, v22, v20\n"
"v_lshl_add_u32 v10, v22, 4, v21\n"
"v_or_b32 v22, 6, v3\n"
"v_xor_b32 v22, v22, v20\n"
"v_lshl_add_u32 v11, v22, 4, v21\n"
"v_bfe_u32 v97, v1, 4, 1\n"
"v_lshlrev_b32 v97, 31, v97\n"
"v_or_b32 v97, 1.0, v97\n"
"v_mov_b32 v98, 0\n"
"v_mov_b32 v99, 1\n"
"s_add_u32 m0, s74, 50176\n"
"s_add_u32 s35, s76, s34\n"
"buffer_load_dwordx4 v4, s[4:7], s35 offen lds\n"
"s_mul_i32 s32, s46, 43008\n"
"s_lshl_b32 s71, s60, 4\n"
"s_add_u32 s32, s32, s71\n"
"s_lshl_b32 s70, s47, 5\n"
"s_add_u32 s70, s70, s46\n"
"s_mul_i32 s80, s70, 58368\n"
"s_add_u32 m0, s74, 54272\n"
"s_add_u32 s35, s77, s34\n"
"buffer_load_dwordx4 v4, s[4:7], s35 offen lds\n"
"s_lshl_b32 s70, s47, 15\n"
"s_lshl_b32 s71, s46, 12\n"
"s_add_u32 s81, s70, s71\n"
"s_add_u32 s70, s83, s46\n"
"s_lshl_b32 s70, s70, 14\n"
"s_lshl_b32 s71, s47, 13\n"
"s_add_u32 s84, s70, s71\n"
"s_add_u32 s84, s84, 0\n"
"s_add_u32 m0, s74, 58368\n"
"s_add_u32 s35, s1, s34\n"
"buffer_load_dwordx4 v4, s[4:7], s35 offen lds\n"
"s_add_u32 s70, s65, 73728\n"
"s_cmp_lt_u32 s45, 6\n"
"s_cselect_b32 s86, s70, 158720\n"
"s_add_u32 s70, s65, 88064\n"
"s_cmp_lt_u32 s45, 6\n"
"s_cselect_b32 s87, s70, 158720\n"
"s_add_u32 s70, s65, 102400\n"
"s_cmp_lt_u32 s45, 6\n"
"s_cselect_b32 s88, s70, 158720\n"
"s_add_u32 m0, s74, 62464\n"
"s_add_u32 s35, s3, s34\n"
"buffer_load_dwordx4 v4, s[4:7], s35 offen lds\n"
"s_lshl_b32 s70, s56, 2\n"
"s_add_u32 s70, s70, s46\n"
"s_lshl_b32 s70, s70, 4\n"
"s_add_u32 s70, s70, s57\n"
"s_lshl_b32 s70, s70, 1\n"
"s_add_u32 s70, s70, s47\n"
"s_lshl_b32 s70, s70, 10\n"
"s_add_u32 s89, s70, 8060928\n"
"s_add_u32 m0, s74, 66560\n"
"s_mul_i32 s35, s34, 1344\n"
"s_add_u32 s35, s35, s78\n"
"buffer_load_dwordx4 v5, s[48:51], s35 offen lds\n"
"s_mul_i32 s71, s45, 3584\n"
"s_lshl_b32 s72, s79, 1\n"
"s_add_u32 s82, s71, s72\n"
"s_add_u32 s71, s65, 110592\n"
"v_add_u32 v252, s71, v0\n"
"s_mul_i32 s70, s46, 6144\n"
"s_add_u32 s70, s70, 123904\n"
"v_add_u32 v15, s70, v0\n"
"s_add_u32 m0, s74, 70656\n"
"s_nop 0\n"
"buffer_load_dwordx4 v12, s[48:51], s35 offen lds\n"
"s_add_u32 s34, s34, 128\n"
"s_cmp_eq_u32 s34, 896\n"
"s_cselect_b32 s34, 0, s34\n"
"s_mul_i32 s75, s61, 896\n"
"s_waitcnt vmcnt(13)\n"
"s_barrier\n"
"s_add_u32 m0, s74, 74752\n"
"s_add_u32 s35, s76, s34\n"
"buffer_load_dwordx4 v4, s[4:7], s35 offen lds\n"
"ds_read_b128 v[228:231], v15 offset:0\n"
"ds_read_b128 v[232:235], v15 offset:1024\n"
"ds_read_b128 v[236:239], v15 offset:2048\n"
"ds_read_b128 v[240:243], v15 offset:3072\n"
"ds_read_b128 v[244:247], v15 offset:4096\n"
"ds_read_b128 v[248:251], v15 offset:5120\n"
"s_add_u32 m0, s74, 78848\n"
"s_add_u32 s35, s77, s34\n"
"buffer_load_dwordx4 v4, s[4:7], s35 offen lds\n"
"s_mul_i32 s70, s64, 29\n"
"s_and_b32 s70, s70, 1\n"
"s_lshl_b32 s70, s70, 2\n"
"s_sub_u32 s70, 2, s70\n"
"s_waitcnt lgkmcnt(0)\n"
"v_add_u32 v246, s70, v246\n"
"s_add_u32 m0, s74, 82944\n"
"s_add_u32 s35, s1, s34\n"
"buffer_load_dwordx4 v4, s[4:7], s35 offen lds\n"
"v_xor_b32 v228, 128, v228\n"
"s_add_u32 m0, s74, 87040\n"
"s_add_u32 s35, s3, s34\n"
"buffer_load_dwordx4 v4, s[4:7], s35 offen lds\n"
"v_xor_b32 v229, 128, v229\n"
"v_xor_b32 v230, 128, v230\n"
"v_xor_b32 v231, 128, v231\n"
"v_xor_b32 v242, 16, v242\n"
"v_add_u32 v232, 32768, v232\n"
"s_add_u32 m0, s74, 91136\n"
"s_mul_i32 s35, s34, 1344\n"
"s_add_u32 s35, s35, s78\n"
"buffer_load_dwordx4 v5, s[48:51], s35 offen lds\n"
"v_add_u32 v241, 4096, v241\n"
"v_add_u32 v243, 4096, v243\n"
"v_add_u32 v244, 4096, v244\n"
"v_add_u32 v245, 4096, v245\n"
"s_add_u32 m0, s74, 95232\n"
"s_nop 0\n"
"buffer_load_dwordx4 v12, s[48:51], s35 offen lds\n"
"s_add_u32 s34, s34, 128\n"
"s_cmp_eq_u32 s34, 896\n"
"s_cselect_b32 s34, 0, s34\n"
"v_add_u32 v247, 64, v247\n"
"v_add_u32 v248, 4096, v248\n"
"v_add_u32 v250, 4096, v250\n"
"v_add_u32 v251, 4096, v251\n"
"s_waitcnt vmcnt(12)\n"
"s_barrier\n"
"s_add_u32 m0, s74, 99328\n"
"s_add_u32 s35, s76, s34\n"
"buffer_load_dwordx4 v4, s[4:7], s35 offen lds\n"
"s_add_u32 m0, s74, 103424\n"
"s_add_u32 s35, s77, s34\n"
"buffer_load_dwordx4 v4, s[4:7], s35 offen lds\n"
"s_add_u32 m0, s74, 107520\n"
"s_add_u32 s35, s1, s34\n"
"buffer_load_dwordx4 v4, s[4:7], s35 offen lds\n"
"s_add_u32 m0, s74, 111616\n"
"s_add_u32 s35, s3, s34\n"
"buffer_load_dwordx4 v4, s[4:7], s35 offen lds\n"
"s_add_u32 m0, s74, 115712\n"
"s_mul_i32 s35, s34, 1344\n"
"s_add_u32 s35, s35, s78\n"
"buffer_load_dwordx4 v5, s[48:51], s35 offen lds\n"
"s_add_u32 m0, s74, 119808\n"
"s_nop 0\n"
"buffer_load_dwordx4 v12, s[48:51], s35 offen lds\n"
"s_add_u32 s34, s34, 128\n"
"s_cmp_eq_u32 s34, 896\n"
"s_cselect_b32 s34, 0, s34\n"
"v_readfirstlane_b32 s66, v254\n"
"v_readfirstlane_b32 s67, v255\n"
"s_nop 0\n"
"s_add_u32 s66, s66, 0x7f4a7c15\n"
"s_addc_u32 s67, s67, 0x9e3779b9\n"
"v_mov_b32 v254, s66\n"
"v_mov_b32 v255, s67\n"
"s_waitcnt vmcnt(12)\n"
"s_barrier\n"
"s_add_u32 m0, s74, 1024\n"
"s_add_u32 s35, s76, s34\n"
"buffer_load_dwordx4 v4, s[4:7], s35 offen lds\n"
"s_add_u32 m0, s74, 5120\n"
"s_add_u32 s35, s77, s34\n"
"buffer_load_dwordx4 v4, s[4:7], s35 offen lds\n"
"s_add_u32 m0, s74, 9216\n"
"s_add_u32 s35, s1, s34\n"
"buffer_load_dwordx4 v4, s[4:7], s35 offen lds\n"
"s_add_u32 m0, s74, 13312\n"
"s_add_u32 s35, s3, s34\n"
"buffer_load_dwordx4 v4, s[4:7], s35 offen lds\n"
"s_add_u32 m0, s74, 17408\n"
"s_mul_i32 s35, s34, 1344\n"
"s_add_u32 s35, s35, s78\n"
"buffer_load_dwordx4 v5, s[48:51], s35 offen lds\n"
"s_add_u32 m0, s74, 21504\n"
"s_nop 0\n"
"buffer_load_dwordx4 v12, s[48:51], s35 offen lds\n"
"s_add_u32 s34, s34, 128\n"
"s_cmp_eq_u32 s34, 896\n"
"s_cselect_b32 s34, 0, s34\n"
"s_waitcnt vmcnt(12)\n"
"s_barrier\n"
"s_add_u32 m0, s74, 25600\n"
"s_add_u32 s35, s76, s34\n"
"buffer_load_dwordx4 v4, s[4:7], s35 offen lds\n"
"s_add_u32 m0, s74, 29696\n"
"s_add_u32 s35, s77, s34\n"
"buffer_load_dwordx4 v4, s[4:7], s35 offen lds\n"
"s_add_u32 m0, s74, 33792\n"
"s_add_u32 s35, s1, s34\n"
"buffer_load_dwordx4 v4, s[4:7], s35 offen lds\n"
"s_add_u32 m0, s74, 37888\n"
"s_add_u32 s35, s3, s34\n"
"buffer_load_dwordx4 v4, s[4:7], s35 offen lds\n"
"s_add_u32 m0, s74, 41984\n"
"s_mul_i32 s35, s34, 1344\n"
"s_add_u32 s35, s35, s78\n"
"buffer_load_dwordx4 v5, s[48:51], s35 offen lds\n"
"s_add_u32 m0, s74, 46080\n"
"s_nop 0\n"
"buffer_load_dwordx4 v12, s[48:51], s35 offen lds\n"
"s_add_u32 s34, s34, 128\n"
"s_cmp_eq_u32 s34, 896\n"
"s_cselect_b32 s34, 0, s34\n"
"s_add_u32 m0, s74, 123904\n"
"s_nop 0\n"
"buffer_load_dwordx4 v13, s[16:19], s75 offen lds\n"
"s_add_u32 m0, s74, 127872\n"
"s_nop 0\n"
"buffer_load_dwordx4 v13, s[16:19], s75 offen offset:128 lds\n"
"s_add_u32 m0, s74, 131840\n"
"s_nop 0\n"
"buffer_load_dwordx4 v13, s[16:19], s75 offen offset:256 lds\n"
"s_add_u32 m0, s74, 135808\n"
"s_nop 0\n"
"buffer_load_dwordx4 v13, s[16:19], s75 offen offset:384 lds\n"
"s_add_u32 m0, s74, 139776\n"
"s_nop 0\n"
"buffer_load_dwordx4 v13, s[16:19], s75 offen offset:512 lds\n"
"s_add_u32 m0, s74, 143744\n"
"s_nop 0\n"
"buffer_load_dwordx4 v13, s[16:19], s75 offen offset:640 lds\n"
"s_add_u32 m0, s74, 147712\n"
"s_nop 0\n"
"buffer_load_dwordx4 v13, s[16:19], s75 offen offset:768 lds\n"
"s_lshr_b32 s70, s46, 1\n"
"s_lshl_b32 s71, s62, 1\n"
"s_add_u32 s70, s70, s71\n"
"s_mul_i32 s70, s70, 29\n"
"s_add_u32 s71, s70, 29\n"
"s_min_u32 s71, s71, 225\n"
"v_bfe_u32 v80, v1, 4, 1\n"
"v_xor_b32 v80, s70, v80\n"
"v_and_b32 v80, 1, v80\n"
"v_and_b32 v81, 15, v1\n"
"v_lshl_add_u32 v80, v81, 1, v80\n"
"v_add_u32 v80, s70, v80\n"
"v_cmp_gt_u32 vcc, s71, v80\n"
"v_cvt_f32_u32 v80, v80\n"
"v_mul_f32 v80, 0x3b124925, v80\n"
"v_cndmask_b32 v80, 0, v80, vcc\n"
"v_cvt_f32_u32 v81, v3\n"
"v_mul_f32 v81, v81, v80\n"
"s_and_b32 s70, s46, 1\n"
"s_mul_i32 s70, s70, 0x3e800000\n"
"v_mov_b32 v85, 0x41000000\n"
"v_mov_b32 v84, s70\n"
"v_fma_f32 v81, v81, v85, v84\n"
"v_mov_b32 v84, 0x3e800000\n"
"v_cndmask_b32 v81, v84, v81, vcc\n"
"v_mul_f32 v82, 0x41800000, v80\n"
"v_cos_f32 v82, v82\n"
"s_nop 0\n"
"v_add_f32 v82, v82, v82\n"
"v_fmamk_f32 v84, v80, 0x00000000, v81\n"
"v_fmamk_f32 v85, v80, 0x3f800000, v81\n"
"v_cos_f32 v64, v84\n"
"v_cos_f32 v65, v85\n"
"s_nop 0\n"
"v_cvt_pk_bf16_f32 v104, v64, v65\n"
"s_waitcnt vmcnt(19)\n"
"s_barrier\n"
"s_add_u32 m0, s74, 50176\n"
"s_mul_i32 s35, s34, 1344\n"
"s_add_u32 s35, s35, s32\n"
"buffer_load_dwordx4 v14, s[8:11], s35 offen lds\n"
"s_add_u32 s34, s34, 128\n"
"s_cmp_eq_u32 s34, 896\n"
"s_cselect_b32 s34, 0, s34\n"
"v_fmamk_f32 v84, v80, 0x40000000, v81\n"
"v_fmamk_f32 v85, v80, 0x40400000, v81\n"
"v_cos_f32 v66, v84\n"
"v_cos_f32 v67, v85\n"
"s_nop 0\n"
"v_cvt_pk_bf16_f32 v105, v66, v67\n"
"v_fmamk_f32 v84, v80, 0x40800000, v81\n"
"v_fmamk_f32 v85, v80, 0x40a00000, v81\n"
"v_cos_f32 v68, v84\n"
"v_cos_f32 v69, v85\n"
"s_nop 0\n"
"v_cvt_pk_bf16_f32 v106, v68, v69\n"
"v_fmamk_f32 v84, v80, 0x40c00000, v81\n"
"v_fmamk_f32 v85, v80, 0x40e00000, v81\n"
"v_cos_f32 v70, v84\n"
"v_cos_f32 v71, v85\n"
"s_nop 0\n"
"v_cvt_pk_bf16_f32 v107, v70, v71\n"
"v_fmamk_f32 v84, v80, 0x41800000, v81\n"
"v_fmamk_f32 v85, v80, 0x41880000, v81\n"
"v_cos_f32 v72, v84\n"
"v_cos_f32 v73, v85\n"
"s_nop 0\n"
"v_cvt_pk_bf16_f32 v108, v72, v73\n"
"v_fmamk_f32 v84, v80, 0x41900000, v81\n"
"v_fmamk_f32 v85, v80, 0x41980000, v81\n"
"v_cos_f32 v74, v84\n"
"v_cos_f32 v75, v85\n"
"s_nop 0\n"
"v_cvt_pk_bf16_f32 v109, v74, v75\n"
"s_waitcnt vmcnt(14)\n"
"s_barrier\n"
"s_add_u32 m0, s74, 54272\n"
"s_mul_i32 s35, s34, 1344\n"
"s_add_u32 s35, s35, s32\n"
"buffer_load_dwordx4 v14, s[8:11], s35 offen lds\n"
"s_add_u32 s34, s34, 128\n"
"s_cmp_eq_u32 s34, 896\n"
"s_cselect_b32 s34, 0, s34\n"
"s_add_u32 m0, s74, 58368\n"
"s_mul_i32 s35, s34, 1344\n"
"s_add_u32 s35, s35, s32\n"
"buffer_load_dwordx4 v14, s[8:11], s35 offen lds\n"
"s_add_u32 s34, s34, 128\n"
"s_cmp_eq_u32 s34, 896\n"
"s_cselect_b32 s34, 0, s34\n"
"s_add_u32 m0, s74, 62464\n"
"s_mul_i32 s35, s34, 1344\n"
"s_add_u32 s35, s35, s32\n"
"buffer_load_dwordx4 v14, s[8:11], s35 offen lds\n"
"s_add_u32 s34, s34, 128\n"
"s_cmp_eq_u32 s34, 896\n"
"s_cselect_b32 s34, 0, s34\n"
"v_fmamk_f32 v84, v80, 0x41a00000, v81\n"
"v_fmamk_f32 v85, v80, 0x41a80000, v81\n"
"v_cos_f32 v76, v84\n"
"v_cos_f32 v77, v85\n"
"s_nop 0\n"
"v_cvt_pk_bf16_f32 v110, v76, v77\n"
"v_fmamk_f32 v84, v80, 0x41b00000, v81\n"
"v_fmamk_f32 v85, v80, 0x41b80000, v81\n"
"v_cos_f32 v78, v84\n"
"v_cos_f32 v79, v85\n"
"s_nop 0\n"
"v_cvt_pk_bf16_f32 v111, v78, v79\n"
"v_pk_fma_f32 v[64:65], v[72:73], v[82:83], v[64:65] op_sel_hi:[1,0,1] neg_lo:[0,0,1] neg_hi:[0,0,1]\n"
"v_cvt_pk_bf16_f32 v112, v64, v65\n"
"v_pk_fma_f32 v[66:67], v[74:75], v[82:83], v[66:67] op_sel_hi:[1,0,1] neg_lo:[0,0,1] neg_hi:[0,0,1]\n"
"v_cvt_pk_bf16_f32 v113, v66, v67\n"
"v_pk_fma_f32 v[68:69], v[76:77], v[82:83], v[68:69] op_sel_hi:[1,0,1] neg_lo:[0,0,1] neg_hi:[0,0,1]\n"
"v_cvt_pk_bf16_f32 v114, v68, v69\n"
"v_pk_fma_f32 v[70:71], v[78:79], v[82:83], v[70:71] op_sel_hi:[1,0,1] neg_lo:[0,0,1] neg_hi:[0,0,1]\n"
"v_cvt_pk_bf16_f32 v115, v70, v71\n"
"v_pk_fma_f32 v[72:73], v[64:65], v[82:83], v[72:73] op_sel_hi:[1,0,1] neg_lo:[0,0,1] neg_hi:[0,0,1]\n"
"v_cvt_pk_bf16_f32 v116, v72, v73\n"
"v_pk_fma_f32 v[74:75], v[66:67], v[82:83], v[74:75] op_sel_hi:[1,0,1] neg_lo:[0,0,1] neg_hi:[0,0,1]\n"
"v_cvt_pk_bf16_f32 v117, v74, v75\n"
"v_pk_fma_f32 v[76:77], v[68:69], v[82:83], v[76:77] op_sel_hi:[1,0,1] neg_lo:[0,0,1] neg_hi:[0,0,1]\n"
"v_cvt_pk_bf16_f32 v118, v76, v77\n"
"v_pk_fma_f32 v[78:79], v[70:71], v[82:83], v[78:79] op_sel_hi:[1,0,1] neg_lo:[0,0,1] neg_hi:[0,0,1]\n"
"v_cvt_pk_bf16_f32 v119, v78, v79\n"
"v_pk_fma_f32 v[64:65], v[72:73], v[82:83], v[64:65] op_sel_hi:[1,0,1] neg_lo:[0,0,1] neg_hi:[0,0,1]\n"
"v_cvt_pk_bf16_f32 v120, v64, v65\n"
"s_waitcnt vmcnt(11)\n"
"s_barrier\n"
"v_pk_fma_f32 v[66:67], v[74:75], v[82:83], v[66:67] op_sel_hi:[1,0,1] neg_lo:[0,0,1] neg_hi:[0,0,1]\n"
"v_cvt_pk_bf16_f32 v121, v66, v67\n"
"v_pk_fma_f32 v[68:69], v[76:77], v[82:83], v[68:69] op_sel_hi:[1,0,1] neg_lo:[0,0,1] neg_hi:[0,0,1]\n"
"v_cvt_pk_bf16_f32 v122, v68, v69\n"
"v_pk_fma_f32 v[70:71], v[78:79], v[82:83], v[70:71] op_sel_hi:[1,0,1] neg_lo:[0,0,1] neg_hi:[0,0,1]\n"
"v_cvt_pk_bf16_f32 v123, v70, v71\n"
"v_pk_fma_f32 v[72:73], v[64:65], v[82:83], v[72:73] op_sel_hi:[1,0,1] neg_lo:[0,0,1] neg_hi:[0,0,1]\n"
"v_cvt_pk_bf16_f32 v124, v72, v73\n"
"v_pk_fma_f32 v[74:75], v[66:67], v[82:83], v[74:75] op_sel_hi:[1,0,1] neg_lo:[0,0,1] neg_hi:[0,0,1]\n"
"v_cvt_pk_bf16_f32 v125, v74, v75\n"
"v_pk_fma_f32 v[76:77], v[68:69], v[82:83], v[76:77] op_sel_hi:[1,0,1] neg_lo:[0,0,1] neg_hi:[0,0,1]\n"
"v_cvt_pk_bf16_f32 v126, v76, v77\n"
"v_pk_fma_f32 v[78:79], v[70:71], v[82:83], v[78:79] op_sel_hi:[1,0,1] neg_lo:[0,0,1] neg_hi:[0,0,1]\n"
"v_cvt_pk_bf16_f32 v127, v78, v79\n"
"v_pk_fma_f32 v[64:65], v[72:73], v[82:83], v[64:65] op_sel_hi:[1,0,1] neg_lo:[0,0,1] neg_hi:[0,0,1]\n"
"v_cvt_pk_bf16_f32 v128, v64, v65\n"
"v_pk_fma_f32 v[66:67], v[74:75], v[82:83], v[66:67] op_sel_hi:[1,0,1] neg_lo:[0,0,1] neg_hi:[0,0,1]\n"
"v_cvt_pk_bf16_f32 v129, v66, v67\n"
"v_pk_fma_f32 v[68:69], v[76:77], v[82:83], v[68:69] op_sel_hi:[1,0,1] neg_lo:[0,0,1] neg_hi:[0,0,1]\n"
"v_cvt_pk_bf16_f32 v130, v68, v69\n"
"v_pk_fma_f32 v[70:71], v[78:79], v[82:83], v[70:71] op_sel_hi:[1,0,1] neg_lo:[0,0,1] neg_hi:[0,0,1]\n"
"v_cvt_pk_bf16_f32 v131, v70, v71\n"
"v_pk_fma_f32 v[72:73], v[64:65], v[82:83], v[72:73] op_sel_hi:[1,0,1] neg_lo:[0,0,1] neg_hi:[0,0,1]\n"
"v_cvt_pk_bf16_f32 v132, v72, v73\n"
"v_pk_fma_f32 v[74:75], v[66:67], v[82:83], v[74:75] op_sel_hi:[1,0,1] neg_lo:[0,0,1] neg_hi:[0,0,1]\n"
"v_cvt_pk_bf16_f32 v133, v74, v75\n"
"v_pk_fma_f32 v[76:77], v[68:69], v[82:83], v[76:77] op_sel_hi:[1,0,1] neg_lo:[0,0,1] neg_hi:[0,0,1]\n"
"v_cvt_pk_bf16_f32 v134, v76, v77\n"
"v_pk_fma_f32 v[78:79], v[70:71], v[82:83], v[78:79] op_sel_hi:[1,0,1] neg_lo:[0,0,1] neg_hi:[0,0,1]\n"
"v_cvt_pk_bf16_f32 v135, v78, v79\n"
"v_pk_fma_f32 v[64:65], v[72:73], v[82:83], v[64:65] op_sel_hi:[1,0,1] neg_lo:[0,0,1] neg_hi:[0,0,1]\n"
"v_cvt_pk_bf16_f32 v136, v64, v65\n"
"v_pk_fma_f32 v[66:67], v[74:75], v[82:83], v[66:67] op_sel_hi:[1,0,1] neg_lo:[0,0,1] neg_hi:[0,0,1]\n"
"v_cvt_pk_bf16_f32 v137, v66, v67\n"
"v_pk_fma_f32 v[68:69], v[76:77], v[82:83], v[68:69] op_sel_hi:[1,0,1] neg_lo:[0,0,1] neg_hi:[0,0,1]\n"
"v_cvt_pk_bf16_f32 v138, v68, v69\n"
"v_pk_fma_f32 v[70:71], v[78:79], v[82:83], v[70:71] op_sel_hi:[1,0,1] neg_lo:[0,0,1] neg_hi:[0,0,1]\n"
"v_cvt_pk_bf16_f32 v139, v70, v71\n"
"v_pk_fma_f32 v[72:73], v[64:65], v[82:83], v[72:73] op_sel_hi:[1,0,1] neg_lo:[0,0,1] neg_hi:[0,0,1]\n"
"v_cvt_pk_bf16_f32 v140, v72, v73\n"
"v_pk_fma_f32 v[74:75], v[66:67], v[82:83], v[74:75] op_sel_hi:[1,0,1] neg_lo:[0,0,1] neg_hi:[0,0,1]\n"
"v_cvt_pk_bf16_f32 v141, v74, v75\n"
"v_pk_fma_f32 v[76:77], v[68:69], v[82:83], v[76:77] op_sel_hi:[1,0,1] neg_lo:[0,0,1] neg_hi:[0,0,1]\n"
"v_cvt_pk_bf16_f32 v142, v76, v77\n"
"v_pk_fma_f32 v[78:79], v[70:71], v[82:83], v[78:79] op_sel_hi:[1,0,1] neg_lo:[0,0,1] neg_hi:[0,0,1]\n"
"v_cvt_pk_bf16_f32 v143, v78, v79\n"
"v_pk_fma_f32 v[64:65], v[72:73], v[82:83], v[64:65] op_sel_hi:[1,0,1] neg_lo:[0,0,1] neg_hi:[0,0,1]\n"
"v_cvt_pk_bf16_f32 v144, v64, v65\n"
"v_pk_fma_f32 v[66:67], v[74:75], v[82:83], v[66:67] op_sel_hi:[1,0,1] neg_lo:[0,0,1] neg_hi:[0,0,1]\n"
"v_cvt_pk_bf16_f32 v145, v66, v67\n"
"v_pk_fma_f32 v[68:69], v[76:77], v[82:83], v[68:69] op_sel_hi:[1,0,1] neg_lo:[0,0,1] neg_hi:[0,0,1]\n"
"v_cvt_pk_bf16_f32 v146, v68, v69\n"
"v_pk_fma_f32 v[70:71], v[78:79], v[82:83], v[70:71] op_sel_hi:[1,0,1] neg_lo:[0,0,1] neg_hi:[0,0,1]\n"
"v_cvt_pk_bf16_f32 v147, v70, v71\n"
"v_pk_fma_f32 v[72:73], v[64:65], v[82:83], v[72:73] op_sel_hi:[1,0,1] neg_lo:[0,0,1] neg_hi:[0,0,1]\n"
"v_cvt_pk_bf16_f32 v148, v72, v73\n"
"v_pk_fma_f32 v[74:75], v[66:67], v[82:83], v[74:75] op_sel_hi:[1,0,1] neg_lo:[0,0,1] neg_hi:[0,0,1]\n"
"v_cvt_pk_bf16_f32 v149, v74, v75\n"
"v_pk_fma_f32 v[76:77], v[68:69], v[82:83], v[76:77] op_sel_hi:[1,0,1] neg_lo:[0,0,1] neg_hi:[0,0,1]\n"
"v_cvt_pk_bf16_f32 v150, v76, v77\n"
"v_pk_fma_f32 v[78:79], v[70:71], v[82:83], v[78:79] op_sel_hi:[1,0,1] neg_lo:[0,0,1] neg_hi:[0,0,1]\n"
"v_cvt_pk_bf16_f32 v151, v78, v79\n"
"v_pk_fma_f32 v[64:65], v[72:73], v[82:83], v[64:65] op_sel_hi:[1,0,1] neg_lo:[0,0,1] neg_hi:[0,0,1]\n"
"v_cvt_pk_bf16_f32 v152, v64, v65\n"
"v_pk_fma_f32 v[66:67], v[74:75], v[82:83], v[66:67] op_sel_hi:[1,0,1] neg_lo:[0,0,1] neg_hi:[0,0,1]\n"
"v_cvt_pk_bf16_f32 v153, v66, v67\n"
"v_pk_fma_f32 v[68:69], v[76:77], v[82:83], v[68:69] op_sel_hi:[1,0,1] neg_lo:[0,0,1] neg_hi:[0,0,1]\n"
"v_cvt_pk_bf16_f32 v154, v68, v69\n"
"v_pk_fma_f32 v[70:71], v[78:79], v[82:83], v[70:71] op_sel_hi:[1,0,1] neg_lo:[0,0,1] neg_hi:[0,0,1]\n"
"v_cvt_pk_bf16_f32 v155, v70, v71\n"
"v_pk_fma_f32 v[72:73], v[64:65], v[82:83], v[72:73] op_sel_hi:[1,0,1] neg_lo:[0,0,1] neg_hi:[0,0,1]\n"
"v_cvt_pk_bf16_f32 v156, v72, v73\n"
"v_pk_fma_f32 v[74:75], v[66:67], v[82:83], v[74:75] op_sel_hi:[1,0,1] neg_lo:[0,0,1] neg_hi:[0,0,1]\n"
"v_cvt_pk_bf16_f32 v157, v74, v75\n"
"v_pk_fma_f32 v[76:77], v[68:69], v[82:83], v[76:77] op_sel_hi:[1,0,1] neg_lo:[0,0,1] neg_hi:[0,0,1]\n"
"v_cvt_pk_bf16_f32 v158, v76, v77\n"
"v_pk_fma_f32 v[78:79], v[70:71], v[82:83], v[78:79] op_sel_hi:[1,0,1] neg_lo:[0,0,1] neg_hi:[0,0,1]\n"
"v_cvt_pk_bf16_f32 v159, v78, v79\n"
"s_lshr_b32 s70, s46, 1\n"
"s_sub_u32 s70, s70, s46\n"
"s_sub_u32 s70, s70, 2\n"
"s_mul_i32 s70, s70, 58368\n"
"s_and_b32 s71, s46, 1\n"
"s_lshl_b32 s71, s71, 10\n"
"s_add_u32 s70, s70, s71\n"
"v_add_u32 v216, s70, v233\n"
"s_mul_i32 s76, s47, 5120\n"
"s_add_u32 s76, s76, 65536\n"
"v_subrev_u32 v217, 58368, v234\n"
"v_add_u32 v218, s76, v0\n"
"v_lshrrev_b32 v220, 3, v1\n"
"v_and_b32 v223, 7, v1\n"
"v_xor_b32 v219, v223, v220\n"
"v_lshlrev_b32 v219, 4, v219\n"
"v_lshl_add_u32 v219, v220, 7, v219\n"
"s_add_u32 s70, s65, 115712\n"
"v_add_u32 v219, s70, v219\n"
"v_lshlrev_b32 v220, 16, v220\n"
"v_lshl_add_u32 v220, v223, 4, v220\n"
"s_lshl_b32 s70, s46, 19\n"
"s_lshl_b32 s71, s62, 1\n"
"s_add_u32 s71, s71, s47\n"
"s_lshl_b32 s71, s71, 13\n"
"s_add_u32 s70, s70, s71\n"
"s_lshl_b32 s71, s61, 7\n"
"s_add_u32 s70, s70, s71\n"
"s_add_u32 s33, s70, 3866624\n"
"s_lshr_b32 s70, s46, 1\n"
"s_lshl_b32 s70, s70, 12\n"
"s_add_u32 s70, s70, 115712\n"
"s_and_b32 s71, s46, 1\n"
"s_lshl_b32 s71, s71, 2\n"
"v_lshl_or_b32 v221, v3, 1, s71\n"
"v_and_b32 v223, 7, v2\n"
"v_xor_b32 v221, v221, v223\n"
"v_lshl_add_u32 v223, v2, 7, s70\n"
"v_xor_b32 v222, 1, v221\n"
"v_lshl_add_u32 v221, v221, 4, v223\n"
"v_lshl_add_u32 v222, v222, 4, v223\n"
"s_cmp_lt_u32 s45, 6\n"
"s_cselect_b32 s70, 28672, 0x40000000\n"
"v_add_u32 v253, s70, v240\n"
"s_cmp_eq_u32 s47, 1\n"
"s_cselect_b32 s48, s40, s36\n"
"s_cselect_b32 s49, s41, s37\n"
"s_mov_b32 s50, s38\n"
"s_mov_b32 s51, s39\n"
"v_mov_b32 v160, 0\n"
"v_mov_b32 v161, 0\n"
"v_mov_b32 v162, 0\n"
"v_mov_b32 v163, 0\n"
"v_mov_b32 v164, 0\n"
"v_mov_b32 v165, 0\n"
"v_mov_b32 v166, 0\n"
"v_mov_b32 v167, 0\n"
"v_mov_b32 v4, -1\n"
"v_mov_b32 v5, -1\n"
"v_mov_b32 v6, -1\n"
"v_mov_b32 v7, -1\n"
"s_waitcnt vmcnt(0)\n"
"s_mov_b64 exec, 1\n"
"ds_add_u32 v98, v99 offset:4\n"
"s_mov_b64 exec, -1\n"
"s_mov_b64 exec, 1\n"
"ds_add_u32 v98, v99 offset:8\n"
"s_mov_b64 exec, -1\n"
"v_mov_b32 v52, 0\n"
"v_lshl_add_u32 v40, v2, 6, s61\n"
"v_cvt_f32_u32 v40, v40\n"
"v_mul_f32 v40, 0x3a000000, v40\n"
"s_and_b32 s70, s46, 1\n"
"s_lshr_b32 s71, s46, 1\n"
"s_lshl_b32 s72, s70, 4\n"
"s_lshl_b32 s73, s71, 2\n"
"s_add_u32 s72, s72, s73\n"
"v_lshl_add_u32 v41, v3, 3, s72\n"
"v_cvt_f32_u32 v41, v41\n"
"v_mul_f32 v41, v41, v40\n"
"v_cos_f32 v44, v41\n"
"v_sin_f32 v48, v41\n"
"v_add_f32 v41, v41, v40\n"
"v_cos_f32 v45, v41\n"
"v_sin_f32 v49, v41\n"
"v_add_f32 v41, v41, v40\n"
"v_cos_f32 v46, v41\n"
"v_sin_f32 v50, v41\n"
"v_add_f32 v41, v41, v40\n"
"v_cos_f32 v47, v41\n"
"v_sin_f32 v51, v41\n"
"v_cvt_pk_bf16_f32 v44, v44, v45\n"
"v_cvt_pk_bf16_f32 v45, v46, v47\n"
"v_cvt_pk_bf16_f32 v46, v48, v49\n"
"v_cvt_pk_bf16_f32 v47, v50, v51\n"
"v_xor_b32 v48, 0x80008000, v46\n"
"v_xor_b32 v49, 0x80008000, v47\n"
"s_lshl_b32 s70, s70, 10\n"
"s_lshl_b32 s71, s71, 3\n"
"s_add_u32 s70, s70, s71\n"
"s_add_u32 s70, s70, 152576\n"
"v_add_u32 v42, s70, v0\n"
"ds_write_b64 v42, v[44:45]\n"
"ds_write_b64 v42, v[46:47] offset:2048\n"
"ds_write_b64 v42, v[48:49] offset:4096\n"
"s_cmp_lg_u32 s46, 0\n"
"s_cbranch_scc1 L_pfs_21_%=\n"
"L_sp_22_%=:\n"
"ds_read_b32 v54, v52 offset:12\n"
"s_waitcnt lgkmcnt(0)\n"
"v_readfirstlane_b32 s71, v54\n"
"s_nop 1\n"
"s_cmp_ge_u32 s71, 4\n"
"s_cbranch_scc1 L_sd_23_%=\n"
"s_cmp_eq_u32 s85, 0\n"
"s_cbranch_scc1 L_sd_23_%=\n"
"s_sub_u32 s85, s85, 1\n"
"s_branch L_sp_22_%=\n"
"L_sd_23_%=:\n"
"s_lshr_b32 s70, s69, 1\n"
"s_mul_i32 s70, s70, 57600\n"
"s_mul_i32 s71, s68, 1800\n"
"s_add_u32 s70, s70, s71\n"
"v_lshrrev_b32 v55, 4, v1\n"
"v_mul_u32_u24 v55, 14400, v55\n"
"v_and_b32 v226, 15, v1\n"
"v_lshl_add_u32 v55, v226, 7, v55\n"
"s_bitcmp1_b32 s69, 0\n"
"s_cselect_b32 s92, s40, s36\n"
"s_cselect_b32 s93, s41, s37\n"
"s_mov_b32 s94, s38\n"
"s_mov_b32 s95, s39\n"
"buffer_load_dword v226, v55, s[92:95], s70 offen\n"
"L_pfs_21_%=:\n"
"L_sp_24_%=:\n"
"ds_read_b32 v54, v52 offset:8\n"
"s_waitcnt lgkmcnt(0)\n"
"v_readfirstlane_b32 s71, v54\n"
"s_nop 1\n"
"s_cmp_ge_u32 s71, 4\n"
"s_cbranch_scc1 L_sd_25_%=\n"
"s_cmp_eq_u32 s85, 0\n"
"s_cbranch_scc1 L_sd_25_%=\n"
"s_sub_u32 s85, s85, 1\n"
"s_branch L_sp_24_%=\n"
"L_sd_25_%=:\n"
"ds_read_b128 v[48:51], v8 offset:0\n"
"ds_read_b128 v[52:55], v10 offset:12288\n"
"ds_read_b128 v[56:59], v9 offset:0\n"
"ds_read_b128 v[60:63], v11 offset:12288\n"
"s_waitcnt lgkmcnt(3)\n"
"v_mfma_f32_32x32x16_bf16 v[16:31], v[48:51], v[104:107], 0\n"
"ds_read_b128 v[48:51], v10 offset:0\n"
"s_waitcnt lgkmcnt(3)\n"
"v_mfma_f32_32x32x16_bf16 v[32:47], v[52:55], v[104:107], 0\n"
"ds_read_b128 v[52:55], v8 offset:16384\n"
"s_waitcnt lgkmcnt(3)\n"
"v_mfma_f32_32x32x16_bf16 v[16:31], v[56:59], v[108:111], v[16:31]\n"
"ds_read_b128 v[56:59], v11 offset:0\n"
"s_waitcnt lgkmcnt(3)\n"
"v_mfma_f32_32x32x16_bf16 v[32:47], v[60:63], v[108:111], v[32:47]\n"
"ds_read_b128 v[60:63], v9 offset:16384\n"
"s_waitcnt lgkmcnt(3)\n"
"v_mfma_f32_32x32x16_bf16 v[16:31], v[48:51], v[112:115], v[16:31]\n"
"ds_read_b128 v[48:51], v8 offset:4096\n"
"s_waitcnt lgkmcnt(3)\n"
"v_mfma_f32_32x32x16_bf16 v[32:47], v[52:55], v[112:115], v[32:47]\n"
"ds_read_b128 v[52:55], v10 offset:16384\n"
"s_waitcnt lgkmcnt(3)\n"
"v_mfma_f32_32x32x16_bf16 v[16:31], v[56:59], v[116:119], v[16:31]\n"
"ds_read_b128 v[56:59], v9 offset:4096\n"
"s_waitcnt lgkmcnt(3)\n"
"v_mfma_f32_32x32x16_bf16 v[32:47], v[60:63], v[116:119], v[32:47]\n"
"ds_read_b128 v[60:63], v11 offset:16384\n"
"s_waitcnt lgkmcnt(3)\n"
"v_mfma_f32_32x32x16_bf16 v[16:31], v[48:51], v[120:123], v[16:31]\n"
"ds_read_b128 v[48:51], v10 offset:4096\n"
"s_waitcnt lgkmcnt(3)\n"
"v_mfma_f32_32x32x16_bf16 v[32:47], v[52:55], v[120:123], v[32:47]\n"
"ds_read_b128 v[52:55], v8 offset:20480\n"
"s_waitcnt lgkmcnt(3)\n"
"v_mfma_f32_32x32x16_bf16 v[16:31], v[56:59], v[124:127], v[16:31]\n"
"ds_read_b128 v[56:59], v11 offset:4096\n"
"s_waitcnt lgkmcnt(3)\n"
"v_mfma_f32_32x32x16_bf16 v[32:47], v[60:63], v[124:127], v[32:47]\n"
"ds_read_b128 v[60:63], v9 offset:20480\n"
"s_waitcnt lgkmcnt(3)\n"
"v_mfma_f32_32x32x16_bf16 v[16:31], v[48:51], v[128:131], v[16:31]\n"
"ds_read_b128 v[48:51], v8 offset:8192\n"
"s_waitcnt lgkmcnt(3)\n"
"v_mfma_f32_32x32x16_bf16 v[32:47], v[52:55], v[128:131], v[32:47]\n"
"ds_read_b128 v[52:55], v10 offset:20480\n"
"s_waitcnt lgkmcnt(3)\n"
"v_mfma_f32_32x32x16_bf16 v[16:31], v[56:59], v[132:135], v[16:31]\n"
"ds_read_b128 v[56:59], v9 offset:8192\n"
"s_waitcnt lgkmcnt(3)\n"
"v_mfma_f32_32x32x16_bf16 v[32:47], v[60:63], v[132:135], v[32:47]\n"
"ds_read_b128 v[60:63], v11 offset:20480\n"
"s_waitcnt lgkmcnt(3)\n"
"v_mfma_f32_32x32x16_bf16 v[16:31], v[48:51], v[136:139], v[16:31]\n"
"ds_read_b128 v[48:51], v10 offset:8192\n"
"s_waitcnt lgkmcnt(3)\n"
"v_mfma_f32_32x32x16_bf16 v[32:47], v[52:55], v[136:139], v[32:47]\n"
"ds_read_b128 v[52:55], v8 offset:24576\n"
"s_waitcnt lgkmcnt(3)\n"
"v_mfma_f32_32x32x16_bf16 v[16:31], v[56:59], v[140:143], v[16:31]\n"
"ds_read_b128 v[56:59], v11 offset:8192\n"
"s_waitcnt lgkmcnt(3)\n"
"v_mfma_f32_32x32x16_bf16 v[32:47], v[60:63], v[140:143], v[32:47]\n"
"ds_read_b128 v[60:63], v9 offset:24576\n"
"s_waitcnt lgkmcnt(3)\n"
"v_mfma_f32_32x32x16_bf16 v[16:31], v[48:51], v[144:147], v[16:31]\n"
"ds_read_b128 v[48:51], v8 offset:12288\n"
"s_waitcnt lgkmcnt(3)\n"
"v_mfma_f32_32x32x16_bf16 v[32:47], v[52:55], v[144:147], v[32:47]\n"
"ds_read_b128 v[52:55], v10 offset:24576\n"
"s_waitcnt lgkmcnt(3)\n"
"v_mfma_f32_32x32x16_bf16 v[16:31], v[56:59], v[148:151], v[16:31]\n"
"ds_read_b128 v[56:59], v9 offset:12288\n"
"s_waitcnt lgkmcnt(3)\n"
"v_mfma_f32_32x32x16_bf16 v[32:47], v[60:63], v[148:151], v[32:47]\n"
"ds_read_b128 v[60:63], v11 offset:24576\n"
"s_waitcnt lgkmcnt(3)\n"
"v_mfma_f32_32x32x16_bf16 v[16:31], v[48:51], v[152:155], v[16:31]\n"
"s_waitcnt lgkmcnt(2)\n"
"v_mfma_f32_32x32x16_bf16 v[32:47], v[52:55], v[152:155], v[32:47]\n"
"s_waitcnt lgkmcnt(1)\n"
"v_mfma_f32_32x32x16_bf16 v[16:31], v[56:59], v[156:159], v[16:31]\n"
"s_waitcnt lgkmcnt(0)\n"
"v_mfma_f32_32x32x16_bf16 v[32:47], v[60:63], v[156:159], v[32:47]\n"
"s_lshl_b32 s75, s46, 10\n"
"s_nop 10\n"
"v_fma_f32 v16, v32, v97, v16\n"
"v_fma_f32 v17, v33, v97, v17\n"
"v_fma_f32 v18, v34, v97, v18\n"
"v_fma_f32 v19, v35, v97, v19\n"
"v_fma_f32 v20, v36, v97, v20\n"
"v_fma_f32 v21, v37, v97, v21\n"
"v_fma_f32 v22, v38, v97, v22\n"
"v_fma_f32 v23, v39, v97, v23\n"
"v_fma_f32 v24, v40, v97, v24\n"
"v_fma_f32 v25, v41, v97, v25\n"
"v_fma_f32 v26, v42, v97, v26\n"
"v_fma_f32 v27, v43, v97, v27\n"
"v_fma_f32 v28, v44, v97, v28\n"
"v_fma_f32 v29, v45, v97, v29\n"
"v_fma_f32 v30, v46, v97, v30\n"
"v_fma_f32 v31, v47, v97, v31\n"
"s_and_b32 s71, s46, 1\n"
"s_lshl_b32 s71, s71, 31\n"
"v_cvt_f32_u32 v40, v2\n"
"v_xor_b32 v40, s71, v40\n"
"v_lshl_add_u32 v41, v3, 8, s61\n"
"v_cvt_f32_u32 v41, v41\n"
"v_mul_f32 v41, 0x3a000000, v41\n"
"v_mul_f32 v41, v41, v40\n"
"v_fmamk_f32 v42, v40, 0x00000000, v41\n"
"v_cos_f32 v80, v42\n"
"v_sin_f32 v88, v42\n"
"v_fmamk_f32 v42, v40, 0x3d000000, v41\n"
"v_cos_f32 v81, v42\n"
"v_sin_f32 v89, v42\n"
"v_fmamk_f32 v42, v40, 0x3d800000, v41\n"
"v_cos_f32 v82, v42\n"
"v_sin_f32 v90, v42\n"
"v_fmamk_f32 v42, v40, 0x3dc00000, v41\n"
"v_cos_f32 v83, v42\n"
"v_sin_f32 v91, v42\n"
"v_fmamk_f32 v42, v40, 0x3e800000, v41\n"
"v_cos_f32 v84, v42\n"
"v_sin_f32 v92, v42\n"
"v_fmamk_f32 v42, v40, 0x3e900000, v41\n"
"v_cos_f32 v85, v42\n"
"v_sin_f32 v93, v42\n"
"v_fmamk_f32 v42, v40, 0x3ea00000, v41\n"
"v_cos_f32 v86, v42\n"
"v_sin_f32 v94, v42\n"
"v_fmamk_f32 v42, v40, 0x3eb00000, v41\n"
"v_cos_f32 v87, v42\n"
"v_sin_f32 v95, v42\n"
"v_and_b32 v42, 1, v1\n"
"v_mul_u32_u24 v42, 0x8000, v42\n"
"v_lshl_or_b32 v42, v42, 16, v42\n"
"v_cvt_pk_bf16_f32 v64, v80, v81\n"
"v_cvt_pk_bf16_f32 v72, v88, v89\n"
"v_cvt_pk_bf16_f32 v65, v82, v83\n"
"v_cvt_pk_bf16_f32 v73, v90, v91\n"
"v_cvt_pk_bf16_f32 v66, v84, v85\n"
"v_cvt_pk_bf16_f32 v74, v92, v93\n"
"v_cvt_pk_bf16_f32 v67, v86, v87\n"
"v_cvt_pk_bf16_f32 v75, v94, v95\n"
"v_xor_b32 v68, v42, v64\n"
"v_xor_b32 v76, v42, v72\n"
"v_xor_b32 v69, v42, v65\n"
"v_xor_b32 v77, v42, v73\n"
"v_xor_b32 v70, v42, v66\n"
"v_xor_b32 v78, v42, v74\n"
"v_xor_b32 v71, v42, v67\n"
"v_xor_b32 v79, v42, v75\n"
"v_cvt_pk_bf16_f32 v80, v16, v17\n"
"v_cvt_pk_bf16_f32 v81, v18, v19\n"
"v_cvt_pk_bf16_f32 v82, v20, v21\n"
"v_cvt_pk_bf16_f32 v83, v22, v23\n"
"v_cvt_pk_bf16_f32 v84, v24, v25\n"
"v_cvt_pk_bf16_f32 v85, v26, v27\n"
"v_cvt_pk_bf16_f32 v86, v28, v29\n"
"v_cvt_pk_bf16_f32 v87, v30, v31\n"
"s_lshl_b32 s70, s46, 11\n"
"s_add_u32 s71, s70, 107520\n"
"v_add_u32 v42, s71, v0\n"
"ds_write_b128 v42, v[80:83]\n"
"ds_write_b128 v42, v[84:87] offset:1024\n"
"s_xor_b32 s71, s70, 2048\n"
"s_add_u32 s71, s71, 107520\n"
"v_add_u32 v43, s71, v0\n"
"s_add_u32 s71, s70, 99328\n"
"v_add_u32 v44, s71, v0\n"
"s_lshl_b32 s71, s70, 1\n"
"s_add_u32 s71, s71, 78848\n"
"v_add_u32 v45, s71, v0\n"
"s_waitcnt lgkmcnt(0)\n"
"s_branch L_j2_4_%=\n"
"L_q_26_%=:\n"
"v_and_b32 v2, 31, v1\n"
"v_lshrrev_b32 v3, 5, v1\n"
"v_bfe_u32 v6, v1, 2, 2\n"
"v_and_b32 v7, 3, v1\n"
"v_bfe_u32 v16, v1, 4, 1\n"
"v_lshrrev_b32 v17, 1, v7\n"
"v_and_b32 v20, 1, v7\n"
"v_lshl_or_b32 v17, v20, 1, v17\n"
"v_and_b32 v18, 15, v1\n"
"v_lshrrev_b32 v19, 4, v1\n"
"v_lshl_add_u32 v23, v3, 3, v6\n"
"s_lshl_b32 s70, s46, 12\n"
"v_bfe_u32 v20, v2, 1, 3\n"
"v_lshl_add_u32 v21, v2, 7, s70\n"
"v_or_b32 v22, 0, v3\n"
"v_xor_b32 v22, v22, v20\n"
"v_lshl_add_u32 v8, v22, 4, v21\n"
"v_or_b32 v22, 2, v3\n"
"v_xor_b32 v22, v22, v20\n"
"v_lshl_add_u32 v9, v22, 4, v21\n"
"v_or_b32 v22, 4, v3\n"
"v_xor_b32 v22, v22, v20\n"
"v_lshl_add_u32 v10, v22, 4, v21\n"
"v_or_b32 v22, 6, v3\n"
"v_xor_b32 v22, v22, v20\n"
"v_lshl_add_u32 v11, v22, 4, v21\n"
"s_cmp_eq_u32 s58, 0\n"
"s_cselect_b64 vcc, -1, 0\n"
"v_cndmask_b32 v22, v7, v17, vcc\n"
"v_lshlrev_b32 v22, 3, v22\n"
"v_lshl_or_b32 v22, v16, 5, v22\n"
"v_lshrrev_b32 v21, 1, v6\n"
"v_lshl_or_b32 v22, v21, 6, v22\n"
"v_lshl_add_u32 v12, v23, 7, v22\n"
"v_add_u32 v12, 16384, v12\n"
"v_xor_b32 v13, 64, v12\n"
"v_lshlrev_b32 v15, 3, v17\n"
"v_lshl_or_b32 v15, v16, 5, v15\n"
"v_lshl_add_u32 v15, v23, 6, v15\n"
"v_add_u32 v15, 50176, v15\n"
"v_mov_b32 v52, 0\n"
"v_mov_b32 v53, 0\n"
"ds_write_b64 v52, v[52:53] offset:0\n"
"ds_write_b64 v52, v[52:53] offset:8\n"
"ds_write_b64 v52, v[52:53] offset:16\n"
"s_waitcnt lgkmcnt(0)\n"
"s_mov_b32 s14, 10158080\n"
"s_mov_b32 s15, 0x20000\n"
"buffer_load_dwordx2 v[254:255], off, s[12:15], 0 sc1\n"
"s_mov_b32 s10, 1204224\n"
"s_mov_b32 s11, 0x20000\n"
"s_mov_b32 s18, 1835008\n"
"s_mov_b32 s19, 0x20000\n"
"s_mov_b32 s22, 229376\n"
"s_mov_b32 s23, 0x20000\n"
"s_mov_b32 s26, 401408\n"
"s_mov_b32 s27, 0x20000\n"
"s_mov_b32 s30, 3670016\n"
"s_mov_b32 s31, 0x20000\n"
"s_mov_b32 s38, 921600\n"
"s_mov_b32 s39, 0x20000\n"
"s_mov_b32 s42, 921600\n"
"s_mov_b32 s43, 0x20000\n"
"s_mov_b32 s52, s12\n"
"s_mov_b32 s53, s13\n"
"s_mov_b32 s55, 0x20000\n"
"s_mov_b32 s85, 200000\n"
"s_lshl_b32 s70, s68, 3\n"
"s_lshr_b32 s71, s69, 2\n"
"s_add_u32 s61, s70, s71\n"
"s_and_b32 s62, s69, 3\n"
"s_lshl_b32 s70, s68, 2\n"
"s_lshr_b32 s71, s69, 3\n"
"s_add_u32 s63, s70, s71\n"
"s_and_b32 s64, s69, 7\n"
"s_mul_i32 s60, s57, 7\n"
"s_lshr_b32 s60, s60, 1\n"
"s_lshl_b32 s79, s62, 7\n"
"s_min_u32 s79, s79, 320\n"
"s_lshl_b32 s83, s62, 2\n"
"s_min_u32 s83, s83, 10\n"
"s_cmp_lg_u32 s46, 0\n"
"s_cbranch_scc1 L_wc_8_%=\n"
"s_mul_i32 s70, s44, 7\n"
"s_lshr_b32 s70, s70, 2\n"
"s_mul_i32 s70, s70, 2688\n"
"v_bfe_u32 v225, v1, 3, 1\n"
"v_mul_u32_u24 v225, 2688, v225\n"
"v_and_b32 v224, 7, v1\n"
"v_min_u32 v224, 6, v224\n"
"v_lshl_add_u32 v225, v224, 7, v225\n"
"buffer_load_dword v224, v225, s[8:11], s70 offen\n"
"s_branch L_we_7_%=\n"
"L_wc_8_%=:\n"
"s_cmp_lg_u32 s46, 1\n"
"s_cbranch_scc1 L_wc_9_%=\n"
"s_mul_i32 s70, s44, 7168\n"
"s_add_u32 s70, s70, 768\n"
"v_and_b32 v225, 7, v1\n"
"v_mul_u32_u24 v225, 896, v225\n"
"s_mov_b32 s6, 1835008\n"
"s_mov_b32 s7, 0x20000\n"
"buffer_load_dword v224, v225, s[4:7], s70 offen\n"
"s_lshr_b32 s70, s44, 2\n"
"s_mul_i32 s70, s70, 2688\n"
"s_and_b32 s71, s44, 3\n"
"s_lshl_b32 s71, s71, 9\n"
"s_add_u32 s70, s70, s71\n"
"s_add_u32 s70, s70, 1033088\n"
"v_and_b32 v225, 3, v1\n"
"v_lshlrev_b32 v225, 7, v225\n"
"buffer_load_dword v224, v225, s[8:11], s70 offen\n"
"s_branch L_we_7_%=\n"
"L_wc_9_%=:\n"
"s_cmp_lg_u32 s46, 3\n"
"s_cbranch_scc1 L_wc_10_%=\n"
"s_mul_i32 s70, s44, 1664\n"
"v_min_u32 v225, 12, v1\n"
"v_lshlrev_b32 v225, 7, v225\n"
"buffer_load_dword v224, v225, s[24:27], s70 offen\n"
"s_mul_i32 s70, s44, 896\n"
"v_min_u32 v225, 768, v225\n"
"buffer_load_dword v224, v225, s[20:23], s70 offen\n"
"s_branch L_we_7_%=\n"
"L_wc_10_%=:\n"
"L_we_7_%=:\n"
"v_lshrrev_b32 v20, 2, v1\n"
"v_mul_u32_u24 v4, 2688, v20\n"
"v_lshl_add_u32 v4, v7, 4, v4\n"
"s_mul_i32 s32, s46, 43008\n"
"s_lshl_b32 s71, s60, 4\n"
"s_add_u32 s32, s32, s71\n"
"s_lshl_b32 s70, s47, 15\n"
"v_add_u32 v232, s70, v0\n"
"s_lshl_b32 s70, s60, 9\n"
"s_add_u32 s70, s70, 247808\n"
"v_add_u32 v233, s70, v0\n"
"s_mul_i32 s70, s57, 28\n"
"s_lshr_b32 s71, s70, 6\n"
"s_lshl_b32 s71, s71, 7\n"
"s_add_u32 s71, s71, 56896\n"
"s_lshl_b32 s70, s70, 4\n"
"s_add_u32 s70, s70, 28672\n"
"v_mul_u32_u24 v20, 7168, v3\n"
"v_lshl_add_u32 v20, v2, 4, v20\n"
"v_add_u32 v20, s70, v20\n"
"v_lshl_add_u32 v21, v2, 4, s71\n"
"v_cmp_gt_u32 vcc, 28, v2\n"
"v_add_u32 v22, 14336, v20\n"
"s_nop 0\n"
"v_cndmask_b32 v20, v21, v20, vcc\n"
"v_cndmask_b32 v22, v21, v22, vcc\n"
"v_add_u32 v234, 187392, v20\n"
"v_add_u32 v235, 128000, v22\n"
"v_lshl_add_u32 v20, v16, 2, v6\n"
"v_xor_b32 v21, s45, v20\n"
"v_lshl_add_u32 v20, v3, 4, v20\n"
"v_lshlrev_b32 v21, 5, v21\n"
"v_lshl_add_u32 v21, v20, 8, v21\n"
"v_lshl_add_u32 v21, v7, 3, v21\n"
"v_add_u32 v228, 65536, v21\n"
"v_cmp_eq_u32 vcc, 3, v19\n"
"v_add_u32 v20, 2048, v228\n"
"v_add_u32 v21, 1024, v228\n"
"v_xor_b32 v21, 128, v21\n"
"v_cndmask_b32 v229, v20, v21, vcc\n"
"v_add_u32 v20, 7168, v228\n"
"v_add_u32 v21, 7168, v229\n"
"v_xor_b32 v230, 128, v20\n"
"v_xor_b32 v231, 128, v21\n"
"s_and_b32 s70, s45, 1\n"
"s_lshl_b32 s70, s70, 2\n"
"v_or_b32 v20, s70, v19\n"
"v_lshrrev_b32 v21, 1, v18\n"
"v_xor_b32 v20, v20, v21\n"
"v_lshlrev_b32 v20, 5, v20\n"
"v_and_b32 v21, 1, v18\n"
"v_lshl_or_b32 v20, v21, 4, v20\n"
"v_mul_u32_u24 v21, 896, v19\n"
"v_add_u32 v240, v20, v21\n"
"v_lshrrev_b32 v20, 3, v1\n"
"v_and_b32 v21, 7, v1\n"
"v_lshlrev_b32 v22, 2, v16\n"
"v_xor_b32 v21, v21, v22\n"
"s_and_b32 s70, s45, 1\n"
"v_xor_b32 v21, s70, v21\n"
"s_lshl_b32 s70, s63, 3\n"
"s_add_u32 s70, s70, s64\n"
"s_lshl_b32 s71, s70, 13\n"
"s_add_u32 s71, s71, s65\n"
"s_add_u32 s71, s71, 3866624\n"
"v_lshlrev_b32 v20, 7, v20\n"
"v_lshl_add_u32 v20, v21, 4, v20\n"
"v_add_u32 v241, s71, v20\n"
"v_and_b32 v20, 1, v7\n"
"v_lshlrev_b32 v20, 1, v20\n"
"v_or_b32 v20, s47, v20\n"
"v_lshrrev_b32 v21, 1, v6\n"
"v_lshlrev_b32 v21, 2, v21\n"
"v_xor_b32 v20, v20, v21\n"
"v_and_b32 v21, 1, v19\n"
"v_xor_b32 v20, v20, v21\n"
"v_lshrrev_b32 v21, 1, v7\n"
"v_lshlrev_b32 v21, 3, v21\n"
"v_lshl_or_b32 v20, v20, 4, v21\n"
"v_lshl_add_u32 v21, v19, 3, v6\n"
"v_lshl_add_u32 v20, v21, 7, v20\n"
"v_add_u32 v242, 123904, v20\n"
"s_lshl_b32 s70, s47, 12\n"
"s_add_u32 s70, s70, 132096\n"
"v_lshl_add_u32 v24, v18, 7, s70\n"
"v_bfe_u32 v25, v18, 1, 3\n"
"s_lshl_b32 s71, s46, 1\n"
"v_lshrrev_b32 v20, 1, v19\n"
"v_or_b32 v20, s71, v20\n"
"v_xor_b32 v20, v20, v25\n"
"v_lshl_add_u32 v20, v20, 4, v24\n"
"v_and_b32 v21, 1, v19\n"
"v_lshl_add_u32 v243, v21, 3, v20\n"
"v_xor_b32 v20, v19, v25\n"
"v_lshl_add_u32 v244, v20, 4, v24\n"
"v_or_b32 v20, 4, v19\n"
"v_xor_b32 v20, v20, v25\n"
"v_lshl_add_u32 v245, v20, 4, v24\n"
"s_mul_i32 s70, s64, 29\n"
"s_xor_b32 s71, s70, s47\n"
"s_and_b32 s71, s71, 1\n"
"s_add_u32 s72, s70, s63\n"
"s_and_b32 s72, s72, 1\n"
"s_add_u32 s71, s71, s72\n"
"s_lshl_b32 s71, s71, 1\n"
"s_lshl_b32 s72, s46, 10\n"
"s_add_u32 s71, s71, s72\n"
"s_add_u32 s71, s71, 140288\n"
"v_lshlrev_b32 v20, 8, v19\n"
"v_lshl_add_u32 v20, v18, 2, v20\n"
"v_add_u32 v246, s71, v20\n"
"s_lshl_b32 s70, s46, 19\n"
"s_lshl_b32 s71, s63, 10\n"
"s_add_u32 s70, s70, s71\n"
"s_lshl_b32 s71, s64, 7\n"
"s_add_u32 s70, s70, s71\n"
"s_lshl_b32 s71, s47, 6\n"
"s_add_u32 s70, s70, s71\n"
"s_add_u32 s70, s70, 5963776\n"
"v_lshlrev_b32 v20, 15, v18\n"
"v_lshl_add_u32 v20, v19, 4, v20\n"
"v_add_u32 v247, s70, v20\n"
"s_bfe_u32 s70, s45, 0x10001\n"
"s_lshl_b32 s70, s70, 7\n"
"s_lshl_b32 s71, s61, 15\n"
"s_add_u32 s71, s71, s65\n"
"s_add_u32 s71, s71, 5963776\n"
"v_xor_b32 v20, s70, v0\n"
"v_add_u32 v248, s71, v20\n"
"s_lshr_b32 s70, s64, 1\n"
"s_lshl_b32 s70, s70, 13\n"
"s_add_u32 s70, s70, s65\n"
"s_add_u32 s70, s70, 65536\n"
"v_and_b32 v20, 7, v1\n"
"v_lshl_add_u32 v250, v20, 7, s70\n"
"s_add_u32 s70, s65, 98304\n"
"v_bfe_u32 v21, v1, 3, 2\n"
"v_lshlrev_b32 v21, 13, v21\n"
"v_lshl_add_u32 v21, v20, 7, v21\n"
"v_add_u32 v251, s70, v21\n"
"s_add_u32 s71, s65, 110592\n"
"v_add_u32 v252, s71, v0\n"
"v_lshlrev_b32 v238, 3, v1\n"
"v_mov_b32 v98, v18\n"
"v_mov_b32 v99, v19\n"
"s_mul_i32 s70, s46, 6144\n"
"s_add_u32 s70, s70, 123904\n"
"v_add_u32 v20, s70, v0\n"
"ds_write_b128 v20, v[228:231] offset:0\n"
"ds_write_b128 v20, v[232:235] offset:1024\n"
"ds_write_b128 v20, v[236:239] offset:2048\n"
"ds_write_b128 v20, v[240:243] offset:3072\n"
"ds_write_b128 v20, v[244:247] offset:4096\n"
"ds_write_b128 v20, v[248:251] offset:5120\n"
"s_waitcnt lgkmcnt(0)\n"
"s_cmp_lg_u32 s58, 0\n"
"s_cbranch_scc1 L_qv_2_%=\n"
"s_waitcnt lgkmcnt(0)\n"
"s_barrier\n"
"ds_read_b128 v[104:107], v8 offset:1024\n"
"ds_read_b64_tr_b16 v[48:49], v12 offset:1024\n"
"ds_read_b64_tr_b16 v[50:51], v12 offset:1536\n"
"ds_read_b64_tr_b16 v[52:53], v13 offset:1024\n"
"ds_read_b64_tr_b16 v[54:55], v13 offset:1536\n"
"ds_read_b128 v[108:111], v9 offset:1024\n"
"ds_read_b64_tr_b16 v[56:57], v12 offset:3072\n"
"ds_read_b64_tr_b16 v[58:59], v12 offset:3584\n"
"ds_read_b64_tr_b16 v[60:61], v13 offset:3072\n"
"ds_read_b64_tr_b16 v[62:63], v13 offset:3584\n"
"ds_read_b128 v[112:115], v10 offset:1024\n"
"ds_read_b64_tr_b16 v[64:65], v12 offset:5120\n"
"ds_read_b64_tr_b16 v[66:67], v12 offset:5632\n"
"ds_read_b64_tr_b16 v[68:69], v13 offset:5120\n"
"ds_read_b64_tr_b16 v[70:71], v13 offset:5632\n"
"s_waitcnt lgkmcnt(10)\n"
"v_mfma_f32_32x32x16_bf16 v[16:31], v[48:51], v[104:107], 0\n"
"v_mfma_f32_32x32x16_bf16 v[32:47], v[52:55], v[104:107], 0\n"
"ds_read_b128 v[116:119], v11 offset:1024\n"
"ds_read_b64_tr_b16 v[72:73], v12 offset:7168\n"
"ds_read_b64_tr_b16 v[74:75], v12 offset:7680\n"
"ds_read_b64_tr_b16 v[76:77], v13 offset:7168\n"
"ds_read_b64_tr_b16 v[78:79], v13 offset:7680\n"
"s_waitcnt lgkmcnt(10)\n"
"v_mfma_f32_32x32x16_bf16 v[16:31], v[56:59], v[108:111], v[16:31]\n"
"v_mfma_f32_32x32x16_bf16 v[32:47], v[60:63], v[108:111], v[32:47]\n"
"s_waitcnt lgkmcnt(5)\n"
"v_mfma_f32_32x32x16_bf16 v[16:31], v[64:67], v[112:115], v[16:31]\n"
"v_mfma_f32_32x32x16_bf16 v[32:47], v[68:71], v[112:115], v[32:47]\n"
"s_waitcnt lgkmcnt(0)\n"
"s_barrier\n"
"ds_read_b128 v[120:123], v8 offset:25600\n"
"ds_read_b64_tr_b16 v[80:81], v12 offset:25600\n"
"ds_read_b64_tr_b16 v[82:83], v12 offset:26112\n"
"ds_read_b64_tr_b16 v[84:85], v13 offset:25600\n"
"ds_read_b64_tr_b16 v[86:87], v13 offset:26112\n"
"ds_read_b128 v[124:127], v9 offset:25600\n"
"ds_read_b64_tr_b16 v[88:89], v12 offset:27648\n"
"ds_read_b64_tr_b16 v[90:91], v12 offset:28160\n"
"ds_read_b64_tr_b16 v[92:93], v13 offset:27648\n"
"ds_read_b64_tr_b16 v[94:95], v13 offset:28160\n"
"v_mfma_f32_32x32x16_bf16 v[16:31], v[72:75], v[116:119], v[16:31]\n"
"v_mfma_f32_32x32x16_bf16 v[32:47], v[76:79], v[116:119], v[32:47]\n"
"ds_read_b128 v[128:131], v10 offset:25600\n"
"ds_read_b64_tr_b16 v[48:49], v12 offset:29696\n"
"ds_read_b64_tr_b16 v[50:51], v12 offset:30208\n"
"ds_read_b64_tr_b16 v[52:53], v13 offset:29696\n"
"ds_read_b64_tr_b16 v[54:55], v13 offset:30208\n"
"s_waitcnt lgkmcnt(10)\n"
"v_mfma_f32_32x32x16_bf16 v[16:31], v[80:83], v[120:123], v[16:31]\n"
"v_mfma_f32_32x32x16_bf16 v[32:47], v[84:87], v[120:123], v[32:47]\n"
"ds_read_b128 v[132:135], v11 offset:25600\n"
"ds_read_b64_tr_b16 v[56:57], v12 offset:31744\n"
"ds_read_b64_tr_b16 v[58:59], v12 offset:32256\n"
"ds_read_b64_tr_b16 v[60:61], v13 offset:31744\n"
"ds_read_b64_tr_b16 v[62:63], v13 offset:32256\n"
"s_waitcnt lgkmcnt(10)\n"
"v_mfma_f32_32x32x16_bf16 v[16:31], v[88:91], v[124:127], v[16:31]\n"
"v_mfma_f32_32x32x16_bf16 v[32:47], v[92:95], v[124:127], v[32:47]\n"
"s_waitcnt lgkmcnt(5)\n"
"v_mfma_f32_32x32x16_bf16 v[16:31], v[48:51], v[128:131], v[16:31]\n"
"v_mfma_f32_32x32x16_bf16 v[32:47], v[52:55], v[128:131], v[32:47]\n"
"s_waitcnt lgkmcnt(0)\n"
"s_barrier\n"
"ds_read_b128 v[136:139], v8 offset:50176\n"
"ds_read_b64_tr_b16 v[64:65], v12 offset:50176\n"
"ds_read_b64_tr_b16 v[66:67], v12 offset:50688\n"
"ds_read_b64_tr_b16 v[68:69], v13 offset:50176\n"
"ds_read_b64_tr_b16 v[70:71], v13 offset:50688\n"
"ds_read_b128 v[140:143], v9 offset:50176\n"
"ds_read_b64_tr_b16 v[72:73], v12 offset:52224\n"
"ds_read_b64_tr_b16 v[74:75], v12 offset:52736\n"
"ds_read_b64_tr_b16 v[76:77], v13 offset:52224\n"
"ds_read_b64_tr_b16 v[78:79], v13 offset:52736\n"
"v_mfma_f32_32x32x16_bf16 v[16:31], v[56:59], v[132:135], v[16:31]\n"
"v_mfma_f32_32x32x16_bf16 v[32:47], v[60:63], v[132:135], v[32:47]\n"
"ds_read_b128 v[144:147], v10 offset:50176\n"
"ds_read_b64_tr_b16 v[80:81], v12 offset:54272\n"
"ds_read_b64_tr_b16 v[82:83], v12 offset:54784\n"
"ds_read_b64_tr_b16 v[84:85], v13 offset:54272\n"
"ds_read_b64_tr_b16 v[86:87], v13 offset:54784\n"
"s_waitcnt lgkmcnt(10)\n"
"v_mfma_f32_32x32x16_bf16 v[16:31], v[64:67], v[136:139], v[16:31]\n"
"v_mfma_f32_32x32x16_bf16 v[32:47], v[68:71], v[136:139], v[32:47]\n"
"ds_read_b128 v[148:151], v11 offset:50176\n"
"ds_read_b64_tr_b16 v[88:89], v12 offset:56320\n"
"ds_read_b64_tr_b16 v[90:91], v12 offset:56832\n"
"ds_read_b64_tr_b16 v[92:93], v13 offset:56320\n"
"ds_read_b64_tr_b16 v[94:95], v13 offset:56832\n"
"s_waitcnt lgkmcnt(10)\n"
"v_mfma_f32_32x32x16_bf16 v[16:31], v[72:75], v[140:143], v[16:31]\n"
"v_mfma_f32_32x32x16_bf16 v[32:47], v[76:79], v[140:143], v[32:47]\n"
"s_waitcnt lgkmcnt(5)\n"
"v_mfma_f32_32x32x16_bf16 v[16:31], v[80:83], v[144:147], v[16:31]\n"
"v_mfma_f32_32x32x16_bf16 v[32:47], v[84:87], v[144:147], v[32:47]\n"
"v_add_u32 v8, 73728, v8\n"
"v_add_u32 v9, 73728, v9\n"
"v_add_u32 v10, 73728, v10\n"
"v_add_u32 v11, 73728, v11\n"
"v_add_u32 v12, 73728, v12\n"
"v_add_u32 v13, 73728, v13\n"
"s_waitcnt vmcnt(0)\n"
"v_readfirstlane_b32 s66, v254\n"
"v_readfirstlane_b32 s67, v255\n"
"s_nop 0\n"
"s_add_u32 s66, s66, 0x7f4a7c15\n"
"s_addc_u32 s67, s67, 0x9e3779b9\n"
"v_mov_b32 v254, s66\n"
"v_mov_b32 v255, s67\n"
"s_waitcnt lgkmcnt(0)\n"
"s_barrier\n"
"ds_read_b128 v[152:155], v8 offset:1024\n"
"ds_read_b64_tr_b16 v[48:49], v12 offset:1024\n"
"ds_read_b64_tr_b16 v[50:51], v12 offset:1536\n"
"ds_read_b64_tr_b16 v[52:53], v13 offset:1024\n"
"ds_read_b64_tr_b16 v[54:55], v13 offset:1536\n"
"ds_read_b128 v[156:159], v9 offset:1024\n"
"ds_read_b64_tr_b16 v[56:57], v12 offset:3072\n"
"ds_read_b64_tr_b16 v[58:59], v12 offset:3584\n"
"ds_read_b64_tr_b16 v[60:61], v13 offset:3072\n"
"ds_read_b64_tr_b16 v[62:63], v13 offset:3584\n"
"v_mfma_f32_32x32x16_bf16 v[16:31], v[88:91], v[148:151], v[16:31]\n"
"v_mfma_f32_32x32x16_bf16 v[32:47], v[92:95], v[148:151], v[32:47]\n"
"ds_read_b128 v[160:163], v10 offset:1024\n"
"ds_read_b64_tr_b16 v[64:65], v12 offset:5120\n"
"ds_read_b64_tr_b16 v[66:67], v12 offset:5632\n"
"ds_read_b64_tr_b16 v[68:69], v13 offset:5120\n"
"ds_read_b64_tr_b16 v[70:71], v13 offset:5632\n"
"s_waitcnt lgkmcnt(10)\n"
"v_mfma_f32_32x32x16_bf16 v[16:31], v[48:51], v[152:155], v[16:31]\n"
"v_mfma_f32_32x32x16_bf16 v[32:47], v[52:55], v[152:155], v[32:47]\n"
"ds_read_b128 v[164:167], v11 offset:1024\n"
"ds_read_b64_tr_b16 v[72:73], v12 offset:7168\n"
"ds_read_b64_tr_b16 v[74:75], v12 offset:7680\n"
"ds_read_b64_tr_b16 v[76:77], v13 offset:7168\n"
"ds_read_b64_tr_b16 v[78:79], v13 offset:7680\n"
"s_waitcnt lgkmcnt(10)\n"
"v_mfma_f32_32x32x16_bf16 v[16:31], v[56:59], v[156:159], v[16:31]\n"
"v_mfma_f32_32x32x16_bf16 v[32:47], v[60:63], v[156:159], v[32:47]\n"
"s_waitcnt lgkmcnt(5)\n"
"v_mfma_f32_32x32x16_bf16 v[16:31], v[64:67], v[160:163], v[16:31]\n"
"v_mfma_f32_32x32x16_bf16 v[32:47], v[68:71], v[160:163], v[32:47]\n"
"s_waitcnt lgkmcnt(0)\n"
"s_barrier\n"
"ds_read_b128 v[168:171], v8 offset:25600\n"
"ds_read_b64_tr_b16 v[80:81], v12 offset:25600\n"
"ds_read_b64_tr_b16 v[82:83], v12 offset:26112\n"
"ds_read_b64_tr_b16 v[84:85], v13 offset:25600\n"
"ds_read_b64_tr_b16 v[86:87], v13 offset:26112\n"
"ds_read_b128 v[172:175], v9 offset:25600\n"
"ds_read_b64_tr_b16 v[88:89], v12 offset:27648\n"
"ds_read_b64_tr_b16 v[90:91], v12 offset:28160\n"
"ds_read_b64_tr_b16 v[92:93], v13 offset:27648\n"
"ds_read_b64_tr_b16 v[94:95], v13 offset:28160\n"
"v_mfma_f32_32x32x16_bf16 v[16:31], v[72:75], v[164:167], v[16:31]\n"
"v_mfma_f32_32x32x16_bf16 v[32:47], v[76:79], v[164:167], v[32:47]\n"
"ds_read_b128 v[176:179], v10 offset:25600\n"
"ds_read_b64_tr_b16 v[48:49], v12 offset:29696\n"
"ds_read_b64_tr_b16 v[50:51], v12 offset:30208\n"
"ds_read_b64_tr_b16 v[52:53], v13 offset:29696\n"
"ds_read_b64_tr_b16 v[54:55], v13 offset:30208\n"
"s_waitcnt lgkmcnt(10)\n"
"v_mfma_f32_32x32x16_bf16 v[16:31], v[80:83], v[168:171], v[16:31]\n"
"v_mfma_f32_32x32x16_bf16 v[32:47], v[84:87], v[168:171], v[32:47]\n"
"ds_read_b128 v[180:183], v11 offset:25600\n"
"ds_read_b64_tr_b16 v[56:57], v12 offset:31744\n"
"ds_read_b64_tr_b16 v[58:59], v12 offset:32256\n"
"ds_read_b64_tr_b16 v[60:61], v13 offset:31744\n"
"ds_read_b64_tr_b16 v[62:63], v13 offset:32256\n"
"s_waitcnt lgkmcnt(10)\n"
"v_mfma_f32_32x32x16_bf16 v[16:31], v[88:91], v[172:175], v[16:31]\n"
"v_mfma_f32_32x32x16_bf16 v[32:47], v[92:95], v[172:175], v[32:47]\n"
"s_waitcnt lgkmcnt(5)\n"
"v_mfma_f32_32x32x16_bf16 v[16:31], v[48:51], v[176:179], v[16:31]\n"
"v_mfma_f32_32x32x16_bf16 v[32:47], v[52:55], v[176:179], v[32:47]\n"
"v_subrev_u32 v8, 73728, v8\n"
"v_subrev_u32 v9, 73728, v9\n"
"v_subrev_u32 v10, 73728, v10\n"
"v_subrev_u32 v11, 73728, v11\n"
"v_subrev_u32 v12, 73728, v12\n"
"v_subrev_u32 v13, 73728, v13\n"
"s_waitcnt lgkmcnt(0)\n"
"s_barrier\n"
"ds_read_b128 v[184:187], v8 offset:1024\n"
"ds_read_b64_tr_b16 v[64:65], v12 offset:1024\n"
"ds_read_b64_tr_b16 v[66:67], v12 offset:1536\n"
"ds_read_b64_tr_b16 v[68:69], v13 offset:1024\n"
"ds_read_b64_tr_b16 v[70:71], v13 offset:1536\n"
"ds_read_b128 v[188:191], v9 offset:1024\n"
"ds_read_b64_tr_b16 v[72:73], v12 offset:3072\n"
"ds_read_b64_tr_b16 v[74:75], v12 offset:3584\n"
"ds_read_b64_tr_b16 v[76:77], v13 offset:3072\n"
"ds_read_b64_tr_b16 v[78:79], v13 offset:3584\n"
"v_mfma_f32_32x32x16_bf16 v[16:31], v[56:59], v[180:183], v[16:31]\n"
"v_mfma_f32_32x32x16_bf16 v[32:47], v[60:63], v[180:183], v[32:47]\n"
"ds_read_b128 v[192:195], v10 offset:1024\n"
"ds_read_b64_tr_b16 v[80:81], v12 offset:5120\n"
"ds_read_b64_tr_b16 v[82:83], v12 offset:5632\n"
"ds_read_b64_tr_b16 v[84:85], v13 offset:5120\n"
"ds_read_b64_tr_b16 v[86:87], v13 offset:5632\n"
"s_waitcnt lgkmcnt(10)\n"
"v_mfma_f32_32x32x16_bf16 v[16:31], v[64:67], v[184:187], v[16:31]\n"
"v_mfma_f32_32x32x16_bf16 v[32:47], v[68:71], v[184:187], v[32:47]\n"
"ds_read_b128 v[196:199], v11 offset:1024\n"
"ds_read_b64_tr_b16 v[88:89], v12 offset:7168\n"
"ds_read_b64_tr_b16 v[90:91], v12 offset:7680\n"
"ds_read_b64_tr_b16 v[92:93], v13 offset:7168\n"
"ds_read_b64_tr_b16 v[94:95], v13 offset:7680\n"
"s_waitcnt lgkmcnt(10)\n"
"v_mfma_f32_32x32x16_bf16 v[16:31], v[72:75], v[188:191], v[16:31]\n"
"v_mfma_f32_32x32x16_bf16 v[32:47], v[76:79], v[188:191], v[32:47]\n"
"s_waitcnt lgkmcnt(5)\n"
"v_mfma_f32_32x32x16_bf16 v[16:31], v[80:83], v[192:195], v[16:31]\n"
"v_mfma_f32_32x32x16_bf16 v[32:47], v[84:87], v[192:195], v[32:47]\n"
"s_waitcnt lgkmcnt(0)\n"
"s_barrier\n"
"ds_read_b128 v[200:203], v8 offset:25600\n"
"ds_read_b64_tr_b16 v[48:49], v12 offset:25600\n"
"ds_read_b64_tr_b16 v[50:51], v12 offset:26112\n"
"ds_read_b64_tr_b16 v[52:53], v13 offset:25600\n"
"ds_read_b64_tr_b16 v[54:55], v13 offset:26112\n"
"ds_read_b128 v[204:207], v9 offset:25600\n"
"ds_read_b64_tr_b16 v[56:57], v12 offset:27648\n"
"ds_read_b64_tr_b16 v[58:59], v12 offset:28160\n"
"ds_read_b64_tr_b16 v[60:61], v13 offset:27648\n"
"ds_read_b64_tr_b16 v[62:63], v13 offset:28160\n"
"v_mfma_f32_32x32x16_bf16 v[16:31], v[88:91], v[196:199], v[16:31]\n"
"v_mfma_f32_32x32x16_bf16 v[32:47], v[92:95], v[196:199], v[32:47]\n"
"ds_read_b128 v[208:211], v10 offset:25600\n"
"ds_read_b64_tr_b16 v[64:65], v12 offset:29696\n"
"ds_read_b64_tr_b16 v[66:67], v12 offset:30208\n"
"ds_read_b64_tr_b16 v[68:69], v13 offset:29696\n"
"ds_read_b64_tr_b16 v[70:71], v13 offset:30208\n"
"s_waitcnt lgkmcnt(10)\n"
"v_mfma_f32_32x32x16_bf16 v[16:31], v[48:51], v[200:203], v[16:31]\n"
"v_mfma_f32_32x32x16_bf16 v[32:47], v[52:55], v[200:203], v[32:47]\n"
"ds_read_b128 v[212:215], v11 offset:25600\n"
"ds_read_b64_tr_b16 v[72:73], v12 offset:31744\n"
"ds_read_b64_tr_b16 v[74:75], v12 offset:32256\n"
"ds_read_b64_tr_b16 v[76:77], v13 offset:31744\n"
"ds_read_b64_tr_b16 v[78:79], v13 offset:32256\n"
"s_waitcnt lgkmcnt(10)\n"
"v_mfma_f32_32x32x16_bf16 v[16:31], v[56:59], v[204:207], v[16:31]\n"
"v_mfma_f32_32x32x16_bf16 v[32:47], v[60:63], v[204:207], v[32:47]\n"
"s_waitcnt lgkmcnt(5)\n"
"v_mfma_f32_32x32x16_bf16 v[16:31], v[64:67], v[208:211], v[16:31]\n"
"v_mfma_f32_32x32x16_bf16 v[32:47], v[68:71], v[208:211], v[32:47]\n"
"s_waitcnt lgkmcnt(0)\n"
"v_mfma_f32_32x32x16_bf16 v[16:31], v[72:75], v[212:215], v[16:31]\n"
"v_mfma_f32_32x32x16_bf16 v[32:47], v[76:79], v[212:215], v[32:47]\n"
"s_lshl_b32 s70, s56, 2\n"
"s_add_u32 s70, s70, s46\n"
"s_mul_i32 s70, s70, 58368\n"
"s_add_u32 s70, s70, 131072\n"
"s_lshl_b32 s71, s59, 12\n"
"s_add_u32 s71, s70, s71\n"
"s_nop 4\n"
"v_cvt_pk_bf16_f32 v48, v16, v17\n"
"v_cvt_pk_bf16_f32 v49, v18, v19\n"
"v_cvt_pk_bf16_f32 v50, v20, v21\n"
"v_cvt_pk_bf16_f32 v51, v22, v23\n"
"buffer_store_dwordx4 v[48:51], v0, s[52:55], s71 offen offset:0 sc1\n"
"v_cvt_pk_bf16_f32 v52, v24, v25\n"
"v_cvt_pk_bf16_f32 v53, v26, v27\n"
"v_cvt_pk_bf16_f32 v54, v28, v29\n"
"v_cvt_pk_bf16_f32 v55, v30, v31\n"
"buffer_store_dwordx4 v[52:55], v0, s[52:55], s71 offen offset:1024 sc1\n"
"v_cvt_pk_bf16_f32 v56, v32, v33\n"
"v_cvt_pk_bf16_f32 v57, v34, v35\n"
"v_cvt_pk_bf16_f32 v58, v36, v37\n"
"v_cvt_pk_bf16_f32 v59, v38, v39\n"
"buffer_store_dwordx4 v[56:59], v0, s[52:55], s71 offen offset:2048 sc1\n"
"v_cvt_pk_bf16_f32 v60, v40, v41\n"
"v_cvt_pk_bf16_f32 v61, v42, v43\n"
"v_cvt_pk_bf16_f32 v62, v44, v45\n"
"v_cvt_pk_bf16_f32 v63, v46, v47\n"
"buffer_store_dwordx4 v[60:63], v0, s[52:55], s71 offen offset:3072 sc1\n"
"s_branch L_qj_3_%=\n"
"L_qv_2_%=:\n"
"s_waitcnt lgkmcnt(0)\n"
"s_barrier\n"
"ds_read_b128 v[104:107], v8 offset:1024\n"
"ds_read_b64_tr_b16 v[48:49], v12 offset:1024\n"
"ds_read_b64_tr_b16 v[50:51], v12 offset:1536\n"
"ds_read_b64_tr_b16 v[52:53], v13 offset:1024\n"
"ds_read_b64_tr_b16 v[54:55], v13 offset:1536\n"
"ds_read_b128 v[108:111], v9 offset:1024\n"
"ds_read_b64_tr_b16 v[56:57], v12 offset:3072\n"
"ds_read_b64_tr_b16 v[58:59], v12 offset:3584\n"
"ds_read_b64_tr_b16 v[60:61], v13 offset:3072\n"
"ds_read_b64_tr_b16 v[62:63], v13 offset:3584\n"
"ds_read_b128 v[112:115], v10 offset:1024\n"
"ds_read_b64_tr_b16 v[64:65], v12 offset:5120\n"
"ds_read_b64_tr_b16 v[66:67], v12 offset:5632\n"
"ds_read_b64_tr_b16 v[68:69], v13 offset:5120\n"
"ds_read_b64_tr_b16 v[70:71], v13 offset:5632\n"
"s_waitcnt lgkmcnt(10)\n"
"v_mfma_f32_32x32x16_bf16 v[16:31], v[104:107], v[48:51], 0\n"
"v_mfma_f32_32x32x16_bf16 v[32:47], v[104:107], v[52:55], 0\n"
"ds_read_b128 v[116:119], v11 offset:1024\n"
"ds_read_b64_tr_b16 v[72:73], v12 offset:7168\n"
"ds_read_b64_tr_b16 v[74:75], v12 offset:7680\n"
"ds_read_b64_tr_b16 v[76:77], v13 offset:7168\n"
"ds_read_b64_tr_b16 v[78:79], v13 offset:7680\n"
"s_waitcnt lgkmcnt(10)\n"
"v_mfma_f32_32x32x16_bf16 v[16:31], v[108:111], v[56:59], v[16:31]\n"
"v_mfma_f32_32x32x16_bf16 v[32:47], v[108:111], v[60:63], v[32:47]\n"
"s_waitcnt lgkmcnt(5)\n"
"v_mfma_f32_32x32x16_bf16 v[16:31], v[112:115], v[64:67], v[16:31]\n"
"v_mfma_f32_32x32x16_bf16 v[32:47], v[112:115], v[68:71], v[32:47]\n"
"s_waitcnt lgkmcnt(0)\n"
"s_barrier\n"
"ds_read_b128 v[120:123], v8 offset:25600\n"
"ds_read_b64_tr_b16 v[80:81], v12 offset:25600\n"
"ds_read_b64_tr_b16 v[82:83], v12 offset:26112\n"
"ds_read_b64_tr_b16 v[84:85], v13 offset:25600\n"
"ds_read_b64_tr_b16 v[86:87], v13 offset:26112\n"
"ds_read_b128 v[124:127], v9 offset:25600\n"
"ds_read_b64_tr_b16 v[88:89], v12 offset:27648\n"
"ds_read_b64_tr_b16 v[90:91], v12 offset:28160\n"
"ds_read_b64_tr_b16 v[92:93], v13 offset:27648\n"
"ds_read_b64_tr_b16 v[94:95], v13 offset:28160\n"
"v_mfma_f32_32x32x16_bf16 v[16:31], v[116:119], v[72:75], v[16:31]\n"
"v_mfma_f32_32x32x16_bf16 v[32:47], v[116:119], v[76:79], v[32:47]\n"
"ds_read_b128 v[128:131], v10 offset:25600\n"
"ds_read_b64_tr_b16 v[48:49], v12 offset:29696\n"
"ds_read_b64_tr_b16 v[50:51], v12 offset:30208\n"
"ds_read_b64_tr_b16 v[52:53], v13 offset:29696\n"
"ds_read_b64_tr_b16 v[54:55], v13 offset:30208\n"
"s_waitcnt lgkmcnt(10)\n"
"v_mfma_f32_32x32x16_bf16 v[16:31], v[120:123], v[80:83], v[16:31]\n"
"v_mfma_f32_32x32x16_bf16 v[32:47], v[120:123], v[84:87], v[32:47]\n"
"ds_read_b128 v[132:135], v11 offset:25600\n"
"ds_read_b64_tr_b16 v[56:57], v12 offset:31744\n"
"ds_read_b64_tr_b16 v[58:59], v12 offset:32256\n"
"ds_read_b64_tr_b16 v[60:61], v13 offset:31744\n"
"ds_read_b64_tr_b16 v[62:63], v13 offset:32256\n"
"s_waitcnt lgkmcnt(10)\n"
"v_mfma_f32_32x32x16_bf16 v[16:31], v[124:127], v[88:91], v[16:31]\n"
"v_mfma_f32_32x32x16_bf16 v[32:47], v[124:127], v[92:95], v[32:47]\n"
"s_waitcnt lgkmcnt(5)\n"
"v_mfma_f32_32x32x16_bf16 v[16:31], v[128:131], v[48:51], v[16:31]\n"
"v_mfma_f32_32x32x16_bf16 v[32:47], v[128:131], v[52:55], v[32:47]\n"
"s_waitcnt lgkmcnt(0)\n"
"s_barrier\n"
"ds_read_b128 v[136:139], v8 offset:50176\n"
"ds_read_b64_tr_b16 v[64:65], v12 offset:50176\n"
"ds_read_b64_tr_b16 v[66:67], v12 offset:50688\n"
"ds_read_b64_tr_b16 v[68:69], v13 offset:50176\n"
"ds_read_b64_tr_b16 v[70:71], v13 offset:50688\n"
"ds_read_b128 v[140:143], v9 offset:50176\n"
"ds_read_b64_tr_b16 v[72:73], v12 offset:52224\n"
"ds_read_b64_tr_b16 v[74:75], v12 offset:52736\n"
"ds_read_b64_tr_b16 v[76:77], v13 offset:52224\n"
"ds_read_b64_tr_b16 v[78:79], v13 offset:52736\n"
"v_mfma_f32_32x32x16_bf16 v[16:31], v[132:135], v[56:59], v[16:31]\n"
"v_mfma_f32_32x32x16_bf16 v[32:47], v[132:135], v[60:63], v[32:47]\n"
"ds_read_b128 v[144:147], v10 offset:50176\n"
"ds_read_b64_tr_b16 v[80:81], v12 offset:54272\n"
"ds_read_b64_tr_b16 v[82:83], v12 offset:54784\n"
"ds_read_b64_tr_b16 v[84:85], v13 offset:54272\n"
"ds_read_b64_tr_b16 v[86:87], v13 offset:54784\n"
"s_waitcnt lgkmcnt(10)\n"
"v_mfma_f32_32x32x16_bf16 v[16:31], v[136:139], v[64:67], v[16:31]\n"
"v_mfma_f32_32x32x16_bf16 v[32:47], v[136:139], v[68:71], v[32:47]\n"
"ds_read_b128 v[148:151], v11 offset:50176\n"
"ds_read_b64_tr_b16 v[88:89], v12 offset:56320\n"
"ds_read_b64_tr_b16 v[90:91], v12 offset:56832\n"
"ds_read_b64_tr_b16 v[92:93], v13 offset:56320\n"
"ds_read_b64_tr_b16 v[94:95], v13 offset:56832\n"
"s_waitcnt lgkmcnt(10)\n"
"v_mfma_f32_32x32x16_bf16 v[16:31], v[140:143], v[72:75], v[16:31]\n"
"v_mfma_f32_32x32x16_bf16 v[32:47], v[140:143], v[76:79], v[32:47]\n"
"s_waitcnt lgkmcnt(5)\n"
"v_mfma_f32_32x32x16_bf16 v[16:31], v[144:147], v[80:83], v[16:31]\n"
"v_mfma_f32_32x32x16_bf16 v[32:47], v[144:147], v[84:87], v[32:47]\n"
"v_add_u32 v8, 73728, v8\n"
"v_add_u32 v9, 73728, v9\n"
"v_add_u32 v10, 73728, v10\n"
"v_add_u32 v11, 73728, v11\n"
"v_add_u32 v12, 73728, v12\n"
"v_add_u32 v13, 73728, v13\n"
"s_waitcnt vmcnt(0)\n"
"v_readfirstlane_b32 s66, v254\n"
"v_readfirstlane_b32 s67, v255\n"
"s_nop 0\n"
"s_add_u32 s66, s66, 0x7f4a7c15\n"
"s_addc_u32 s67, s67, 0x9e3779b9\n"
"v_mov_b32 v254, s66\n"
"v_mov_b32 v255, s67\n"
"s_waitcnt lgkmcnt(0)\n"
"s_barrier\n"
"ds_read_b128 v[152:155], v8 offset:1024\n"
"ds_read_b64_tr_b16 v[48:49], v12 offset:1024\n"
"ds_read_b64_tr_b16 v[50:51], v12 offset:1536\n"
"ds_read_b64_tr_b16 v[52:53], v13 offset:1024\n"
"ds_read_b64_tr_b16 v[54:55], v13 offset:1536\n"
"ds_read_b128 v[156:159], v9 offset:1024\n"
"ds_read_b64_tr_b16 v[56:57], v12 offset:3072\n"
"ds_read_b64_tr_b16 v[58:59], v12 offset:3584\n"
"ds_read_b64_tr_b16 v[60:61], v13 offset:3072\n"
"ds_read_b64_tr_b16 v[62:63], v13 offset:3584\n"
"v_mfma_f32_32x32x16_bf16 v[16:31], v[148:151], v[88:91], v[16:31]\n"
"v_mfma_f32_32x32x16_bf16 v[32:47], v[148:151], v[92:95], v[32:47]\n"
"ds_read_b128 v[160:163], v10 offset:1024\n"
"ds_read_b64_tr_b16 v[64:65], v12 offset:5120\n"
"ds_read_b64_tr_b16 v[66:67], v12 offset:5632\n"
"ds_read_b64_tr_b16 v[68:69], v13 offset:5120\n"
"ds_read_b64_tr_b16 v[70:71], v13 offset:5632\n"
"s_waitcnt lgkmcnt(10)\n"
"v_mfma_f32_32x32x16_bf16 v[16:31], v[152:155], v[48:51], v[16:31]\n"
"v_mfma_f32_32x32x16_bf16 v[32:47], v[152:155], v[52:55], v[32:47]\n"
"ds_read_b128 v[164:167], v11 offset:1024\n"
"ds_read_b64_tr_b16 v[72:73], v12 offset:7168\n"
"ds_read_b64_tr_b16 v[74:75], v12 offset:7680\n"
"ds_read_b64_tr_b16 v[76:77], v13 offset:7168\n"
"ds_read_b64_tr_b16 v[78:79], v13 offset:7680\n"
"s_waitcnt lgkmcnt(10)\n"
"v_mfma_f32_32x32x16_bf16 v[16:31], v[156:159], v[56:59], v[16:31]\n"
"v_mfma_f32_32x32x16_bf16 v[32:47], v[156:159], v[60:63], v[32:47]\n"
"s_waitcnt lgkmcnt(5)\n"
"v_mfma_f32_32x32x16_bf16 v[16:31], v[160:163], v[64:67], v[16:31]\n"
"v_mfma_f32_32x32x16_bf16 v[32:47], v[160:163], v[68:71], v[32:47]\n"
"s_waitcnt lgkmcnt(0)\n"
"s_barrier\n"
"ds_read_b128 v[168:171], v8 offset:25600\n"
"ds_read_b64_tr_b16 v[80:81], v12 offset:25600\n"
"ds_read_b64_tr_b16 v[82:83], v12 offset:26112\n"
"ds_read_b64_tr_b16 v[84:85], v13 offset:25600\n"
"ds_read_b64_tr_b16 v[86:87], v13 offset:26112\n"
"ds_read_b128 v[172:175], v9 offset:25600\n"
"ds_read_b64_tr_b16 v[88:89], v12 offset:27648\n"
"ds_read_b64_tr_b16 v[90:91], v12 offset:28160\n"
"ds_read_b64_tr_b16 v[92:93], v13 offset:27648\n"
"ds_read_b64_tr_b16 v[94:95], v13 offset:28160\n"
"v_mfma_f32_32x32x16_bf16 v[16:31], v[164:167], v[72:75], v[16:31]\n"
"v_mfma_f32_32x32x16_bf16 v[32:47], v[164:167], v[76:79], v[32:47]\n"
"ds_read_b128 v[176:179], v10 offset:25600\n"
"ds_read_b64_tr_b16 v[48:49], v12 offset:29696\n"
"ds_read_b64_tr_b16 v[50:51], v12 offset:30208\n"
"ds_read_b64_tr_b16 v[52:53], v13 offset:29696\n"
"ds_read_b64_tr_b16 v[54:55], v13 offset:30208\n"
"s_waitcnt lgkmcnt(10)\n"
"v_mfma_f32_32x32x16_bf16 v[16:31], v[168:171], v[80:83], v[16:31]\n"
"v_mfma_f32_32x32x16_bf16 v[32:47], v[168:171], v[84:87], v[32:47]\n"
"ds_read_b128 v[180:183], v11 offset:25600\n"
"ds_read_b64_tr_b16 v[56:57], v12 offset:31744\n"
"ds_read_b64_tr_b16 v[58:59], v12 offset:32256\n"
"ds_read_b64_tr_b16 v[60:61], v13 offset:31744\n"
"ds_read_b64_tr_b16 v[62:63], v13 offset:32256\n"
"s_waitcnt lgkmcnt(10)\n"
"v_mfma_f32_32x32x16_bf16 v[16:31], v[172:175], v[88:91], v[16:31]\n"
"v_mfma_f32_32x32x16_bf16 v[32:47], v[172:175], v[92:95], v[32:47]\n"
"s_waitcnt lgkmcnt(5)\n"
"v_mfma_f32_32x32x16_bf16 v[16:31], v[176:179], v[48:51], v[16:31]\n"
"v_mfma_f32_32x32x16_bf16 v[32:47], v[176:179], v[52:55], v[32:47]\n"
"v_subrev_u32 v8, 73728, v8\n"
"v_subrev_u32 v9, 73728, v9\n"
"v_subrev_u32 v10, 73728, v10\n"
"v_subrev_u32 v11, 73728, v11\n"
"v_subrev_u32 v12, 73728, v12\n"
"v_subrev_u32 v13, 73728, v13\n"
"s_waitcnt lgkmcnt(0)\n"
"s_barrier\n"
"ds_read_b128 v[184:187], v8 offset:1024\n"
"ds_read_b64_tr_b16 v[64:65], v12 offset:1024\n"
"ds_read_b64_tr_b16 v[66:67], v12 offset:1536\n"
"ds_read_b64_tr_b16 v[68:69], v13 offset:1024\n"
"ds_read_b64_tr_b16 v[70:71], v13 offset:1536\n"
"ds_read_b128 v[188:191], v9 offset:1024\n"
"ds_read_b64_tr_b16 v[72:73], v12 offset:3072\n"
"ds_read_b64_tr_b16 v[74:75], v12 offset:3584\n"
"ds_read_b64_tr_b16 v[76:77], v13 offset:3072\n"
"ds_read_b64_tr_b16 v[78:79], v13 offset:3584\n"
"v_mfma_f32_32x32x16_bf16 v[16:31], v[180:183], v[56:59], v[16:31]\n"
"v_mfma_f32_32x32x16_bf16 v[32:47], v[180:183], v[60:63], v[32:47]\n"
"ds_read_b128 v[192:195], v10 offset:1024\n"
"ds_read_b64_tr_b16 v[80:81], v12 offset:5120\n"
"ds_read_b64_tr_b16 v[82:83], v12 offset:5632\n"
"ds_read_b64_tr_b16 v[84:85], v13 offset:5120\n"
"ds_read_b64_tr_b16 v[86:87], v13 offset:5632\n"
"s_waitcnt lgkmcnt(10)\n"
"v_mfma_f32_32x32x16_bf16 v[16:31], v[184:187], v[64:67], v[16:31]\n"
"v_mfma_f32_32x32x16_bf16 v[32:47], v[184:187], v[68:71], v[32:47]\n"
"ds_read_b128 v[196:199], v11 offset:1024\n"
"ds_read_b64_tr_b16 v[88:89], v12 offset:7168\n"
"ds_read_b64_tr_b16 v[90:91], v12 offset:7680\n"
"ds_read_b64_tr_b16 v[92:93], v13 offset:7168\n"
"ds_read_b64_tr_b16 v[94:95], v13 offset:7680\n"
"s_waitcnt lgkmcnt(10)\n"
"v_mfma_f32_32x32x16_bf16 v[16:31], v[188:191], v[72:75], v[16:31]\n"
"v_mfma_f32_32x32x16_bf16 v[32:47], v[188:191], v[76:79], v[32:47]\n"
"s_waitcnt lgkmcnt(5)\n"
"v_mfma_f32_32x32x16_bf16 v[16:31], v[192:195], v[80:83], v[16:31]\n"
"v_mfma_f32_32x32x16_bf16 v[32:47], v[192:195], v[84:87], v[32:47]\n"
"s_waitcnt lgkmcnt(0)\n"
"s_barrier\n"
"ds_read_b128 v[200:203], v8 offset:25600\n"
"ds_read_b64_tr_b16 v[48:49], v12 offset:25600\n"
"ds_read_b64_tr_b16 v[50:51], v12 offset:26112\n"
"ds_read_b64_tr_b16 v[52:53], v13 offset:25600\n"
"ds_read_b64_tr_b16 v[54:55], v13 offset:26112\n"
"ds_read_b128 v[204:207], v9 offset:25600\n"
"ds_read_b64_tr_b16 v[56:57], v12 offset:27648\n"
"ds_read_b64_tr_b16 v[58:59], v12 offset:28160\n"
"ds_read_b64_tr_b16 v[60:61], v13 offset:27648\n"
"ds_read_b64_tr_b16 v[62:63], v13 offset:28160\n"
"v_mfma_f32_32x32x16_bf16 v[16:31], v[196:199], v[88:91], v[16:31]\n"
"v_mfma_f32_32x32x16_bf16 v[32:47], v[196:199], v[92:95], v[32:47]\n"
"ds_read_b128 v[208:211], v10 offset:25600\n"
"ds_read_b64_tr_b16 v[64:65], v12 offset:29696\n"
"ds_read_b64_tr_b16 v[66:67], v12 offset:30208\n"
"ds_read_b64_tr_b16 v[68:69], v13 offset:29696\n"
"ds_read_b64_tr_b16 v[70:71], v13 offset:30208\n"
"s_waitcnt lgkmcnt(10)\n"
"v_mfma_f32_32x32x16_bf16 v[16:31], v[200:203], v[48:51], v[16:31]\n"
"v_mfma_f32_32x32x16_bf16 v[32:47], v[200:203], v[52:55], v[32:47]\n"
"ds_read_b128 v[212:215], v11 offset:25600\n"
"ds_read_b64_tr_b16 v[72:73], v12 offset:31744\n"
"ds_read_b64_tr_b16 v[74:75], v12 offset:32256\n"
"ds_read_b64_tr_b16 v[76:77], v13 offset:31744\n"
"ds_read_b64_tr_b16 v[78:79], v13 offset:32256\n"
"s_waitcnt lgkmcnt(10)\n"
"v_mfma_f32_32x32x16_bf16 v[16:31], v[204:207], v[56:59], v[16:31]\n"
"v_mfma_f32_32x32x16_bf16 v[32:47], v[204:207], v[60:63], v[32:47]\n"
"s_waitcnt lgkmcnt(5)\n"
"v_mfma_f32_32x32x16_bf16 v[16:31], v[208:211], v[64:67], v[16:31]\n"
"v_mfma_f32_32x32x16_bf16 v[32:47], v[208:211], v[68:71], v[32:47]\n"
"s_waitcnt lgkmcnt(0)\n"
"v_mfma_f32_32x32x16_bf16 v[16:31], v[212:215], v[72:75], v[16:31]\n"
"v_mfma_f32_32x32x16_bf16 v[32:47], v[212:215], v[76:79], v[32:47]\n"
"s_lshl_b32 s70, s56, 2\n"
"s_add_u32 s70, s70, s46\n"
"s_mul_i32 s70, s70, 58368\n"
"s_add_u32 s70, s70, 131072\n"
"s_lshl_b32 s71, s59, 10\n"
"s_add_u32 s71, s70, s71\n"
"s_add_u32 s71, s71, 28672\n"
"s_add_u32 s72, s71, 14336\n"
"v_mul_u32_u24 v14, 7168, v3\n"
"v_lshl_add_u32 v14, v2, 4, v14\n"
"s_lshl_b32 s73, s59, 7\n"
"s_add_u32 s73, s73, s70\n"
"s_add_u32 s73, s73, 57344\n"
"v_mov_b32 v8, 0x3f803f80\n"
"v_cmp_gt_u32 vcc, 4, v2\n"
"v_lshlrev_b32 v9, 2, v2\n"
"s_nop 0\n"
"v_cndmask_b32 v8, 0, v8, vcc\n"
"v_cvt_pk_bf16_f32 v48, v16, v17\n"
"v_cvt_pk_bf16_f32 v49, v18, v19\n"
"v_cvt_pk_bf16_f32 v50, v20, v21\n"
"v_cvt_pk_bf16_f32 v51, v22, v23\n"
"buffer_store_dwordx4 v[48:51], v14, s[52:55], s71 offen offset:0 sc1\n"
"v_cvt_pk_bf16_f32 v52, v24, v25\n"
"v_cvt_pk_bf16_f32 v53, v26, v27\n"
"v_cvt_pk_bf16_f32 v54, v28, v29\n"
"v_cvt_pk_bf16_f32 v55, v30, v31\n"
"buffer_store_dwordx4 v[52:55], v14, s[52:55], s72 offen offset:0 sc1\n"
"v_cvt_pk_bf16_f32 v56, v32, v33\n"
"v_cvt_pk_bf16_f32 v57, v34, v35\n"
"v_cvt_pk_bf16_f32 v58, v36, v37\n"
"v_cvt_pk_bf16_f32 v59, v38, v39\n"
"buffer_store_dwordx4 v[56:59], v14, s[52:55], s71 offen offset:512 sc1\n"
"v_cvt_pk_bf16_f32 v60, v40, v41\n"
"v_cvt_pk_bf16_f32 v61, v42, v43\n"
"v_cvt_pk_bf16_f32 v62, v44, v45\n"
"v_cvt_pk_bf16_f32 v63, v46, v47\n"
"buffer_store_dwordx4 v[60:63], v14, s[52:55], s72 offen offset:512 sc1\n"
"buffer_store_dword v8, v9, s[52:55], s73 offen sc1\n"
"L_qj_3_%=:\n"
"s_add_u32 s70, s57, s56\n"
"s_and_b32 s70, s70, 1\n"
"s_mul_i32 s70, s70, 3\n"
"s_add_u32 s72, s70, 4\n"
"s_sub_u32 s71, s72, 7\n"
"s_cselect_b32 s72, s72, s71\n"
"s_mul_i32 s72, s72, 172032\n"
"s_add_u32 m0, s65, 66560\n"
"s_add_u32 s72, s32, s72\n"
"buffer_load_dwordx4 v4, s[8:11], s72 offen lds\n"
"s_add_u32 s72, s70, 5\n"
"s_sub_u32 s71, s72, 7\n"
"s_cselect_b32 s72, s72, s71\n"
"s_mul_i32 s72, s72, 172032\n"
"s_add_u32 m0, s65, 70656\n"
"s_add_u32 s72, s32, s72\n"
"buffer_load_dwordx4 v4, s[8:11], s72 offen lds\n"
"s_add_u32 s72, s70, 6\n"
"s_sub_u32 s71, s72, 7\n"
"s_cselect_b32 s72, s72, s71\n"
"s_mul_i32 s72, s72, 172032\n"
"s_add_u32 m0, s65, 74752\n"
"s_add_u32 s72, s32, s72\n"
"buffer_load_dwordx4 v4, s[8:11], s72 offen lds\n"
"v_mov_b32 v52, 0\n"
"v_mov_b32 v53, 1\n"
"s_mov_b64 exec, 1\n"
"ds_add_u32 v52, v53 offset:12\n"
"s_mov_b64 exec, -1\n"
"s_mov_b32 s1, 0\n"
"s_lshr_b32 s70, s60, 3\n"
"s_add_u32 s71, s60, 3\n"
"s_lshr_b32 s71, s71, 3\n"
"s_mul_i32 s72, s57, 28\n"
"s_add_u32 s73, s72, 27\n"
"s_lshr_b32 s72, s72, 6\n"
"s_lshr_b32 s73, s73, 6\n"
"s_add_u32 s72, s72, 8\n"
"s_add_u32 s73, s73, 8\n"
"v_mov_b32 v80, s70\n"
"v_mov_b32 v81, s71\n"
"v_cmp_eq_u32 vcc, 1, v99\n"
"s_nop 1\n"
"v_cndmask_b32 v80, v80, v81, vcc\n"
"v_mov_b32 v81, s72\n"
"v_cmp_eq_u32 vcc, 2, v99\n"
"s_nop 1\n"
"v_cndmask_b32 v80, v80, v81, vcc\n"
"v_mov_b32 v81, s73\n"
"v_cmp_eq_u32 vcc, 3, v99\n"
"s_nop 1\n"
"v_cndmask_b32 v80, v80, v81, vcc\n"
"v_lshl_add_u32 v80, v80, 4, v98\n"
"v_lshlrev_b32 v80, 7, v80\n"
"v_add_u32 v59, 32768, v80\n"
"s_lshl_b32 s70, s46, 4\n"
"v_or_b32 v80, s70, v98\n"
"v_cvt_f32_u32 v80, v80\n"
"v_mul_f32 v80, 0x3c800000, v80\n"
"v_cvt_f32_u32 v81, v99\n"
"v_mul_f32 v81, v81, v80\n"
"v_mul_f32 v81, 0x41000000, v81\n"
"v_fmamk_f32 v82, v80, 0x00000000, v81\n"
"v_cos_f32 v84, v82\n"
"v_sin_f32 v216, v82\n"
"v_fmamk_f32 v82, v80, 0x3f800000, v81\n"
"v_cos_f32 v85, v82\n"
"v_sin_f32 v217, v82\n"
"v_fmamk_f32 v82, v80, 0x40000000, v81\n"
"v_cos_f32 v86, v82\n"
"v_sin_f32 v218, v82\n"
"v_fmamk_f32 v82, v80, 0x40400000, v81\n"
"v_cos_f32 v87, v82\n"
"v_sin_f32 v219, v82\n"
"v_fmamk_f32 v82, v80, 0x40800000, v81\n"
"v_cos_f32 v88, v82\n"
"v_sin_f32 v220, v82\n"
"v_fmamk_f32 v82, v80, 0x40a00000, v81\n"
"v_cos_f32 v89, v82\n"
"v_sin_f32 v221, v82\n"
"v_fmamk_f32 v82, v80, 0x40c00000, v81\n"
"v_cos_f32 v90, v82\n"
"v_sin_f32 v222, v82\n"
"v_fmamk_f32 v82, v80, 0x40e00000, v81\n"
"v_cos_f32 v91, v82\n"
"v_sin_f32 v223, v82\n"
"v_and_b32 v82, 1, v1\n"
"v_mul_u32_u24 v82, 0x8000, v82\n"
"v_lshl_or_b32 v82, v82, 16, v82\n"
"v_cvt_pk_bf16_f32 v64, v84, v85\n"
"v_cvt_pk_bf16_f32 v72, v216, v217\n"
"v_cvt_pk_bf16_f32 v65, v86, v87\n"
"v_cvt_pk_bf16_f32 v73, v218, v219\n"
"v_cvt_pk_bf16_f32 v66, v88, v89\n"
"v_cvt_pk_bf16_f32 v74, v220, v221\n"
"v_cvt_pk_bf16_f32 v67, v90, v91\n"
"v_cvt_pk_bf16_f32 v75, v222, v223\n"
"v_xor_b32 v68, v82, v64\n"
"v_xor_b32 v76, v82, v72\n"
"L_sp_11_%=:\n"
"ds_read_b32 v54, v52 offset:4\n"
"s_waitcnt lgkmcnt(0)\n"
"v_readfirstlane_b32 s71, v54\n"
"s_nop 1\n"
"s_cmp_ge_u32 s71, 4\n"
"s_cbranch_scc1 L_sd_12_%=\n"
"s_cmp_eq_u32 s85, 0\n"
"s_cbranch_scc1 L_sd_12_%=\n"
"s_sub_u32 s85, s85, 1\n"
"s_branch L_sp_11_%=\n"
"L_sd_12_%=:\n"
"ds_read_b64_tr_b16 v[32:33], v15 offset:0\n"
"ds_read_b64_tr_b16 v[34:35], v15 offset:256\n"
"ds_read_b64_tr_b16 v[36:37], v15 offset:1024\n"
"ds_read_b64_tr_b16 v[38:39], v15 offset:1280\n"
"ds_read_b64_tr_b16 v[40:41], v15 offset:2048\n"
"ds_read_b64_tr_b16 v[42:43], v15 offset:2304\n"
"ds_read_b64_tr_b16 v[44:45], v15 offset:3072\n"
"ds_read_b64_tr_b16 v[46:47], v15 offset:3328\n"
"s_waitcnt lgkmcnt(6)\n"
"v_mfma_f32_32x32x16_bf16 v[16:31], v[32:35], v[104:107], 0\n"
"ds_read_b64_tr_b16 v[32:33], v15 offset:4096\n"
"ds_read_b64_tr_b16 v[34:35], v15 offset:4352\n"
"s_waitcnt lgkmcnt(6)\n"
"v_mfma_f32_32x32x16_bf16 v[16:31], v[36:39], v[108:111], v[16:31]\n"
"ds_read_b64_tr_b16 v[36:37], v15 offset:5120\n"
"ds_read_b64_tr_b16 v[38:39], v15 offset:5376\n"
"s_waitcnt lgkmcnt(6)\n"
"v_mfma_f32_32x32x16_bf16 v[16:31], v[40:43], v[112:115], v[16:31]\n"
"ds_read_b64_tr_b16 v[40:41], v15 offset:6144\n"
"ds_read_b64_tr_b16 v[42:43], v15 offset:6400\n"
"s_waitcnt lgkmcnt(6)\n"
"v_mfma_f32_32x32x16_bf16 v[16:31], v[44:47], v[116:119], v[16:31]\n"
"ds_read_b64_tr_b16 v[44:45], v15 offset:7168\n"
"ds_read_b64_tr_b16 v[46:47], v15 offset:7424\n"
"s_waitcnt lgkmcnt(6)\n"
"v_mfma_f32_32x32x16_bf16 v[16:31], v[32:35], v[120:123], v[16:31]\n"
"ds_read_b64_tr_b16 v[32:33], v15 offset:8192\n"
"ds_read_b64_tr_b16 v[34:35], v15 offset:8448\n"
"s_waitcnt lgkmcnt(6)\n"
"v_mfma_f32_32x32x16_bf16 v[16:31], v[36:39], v[124:127], v[16:31]\n"
"ds_read_b64_tr_b16 v[36:37], v15 offset:9216\n"
"ds_read_b64_tr_b16 v[38:39], v15 offset:9472\n"
"s_waitcnt lgkmcnt(6)\n"
"v_mfma_f32_32x32x16_bf16 v[16:31], v[40:43], v[128:131], v[16:31]\n"
"ds_read_b64_tr_b16 v[40:41], v15 offset:10240\n"
"ds_read_b64_tr_b16 v[42:43], v15 offset:10496\n"
"s_waitcnt lgkmcnt(6)\n"
"v_mfma_f32_32x32x16_bf16 v[16:31], v[44:47], v[132:135], v[16:31]\n"
"ds_read_b64_tr_b16 v[44:45], v15 offset:11264\n"
"ds_read_b64_tr_b16 v[46:47], v15 offset:11520\n"
"s_waitcnt lgkmcnt(6)\n"
"v_mfma_f32_32x32x16_bf16 v[16:31], v[32:35], v[136:139], v[16:31]\n"
"ds_read_b64_tr_b16 v[32:33], v15 offset:12288\n"
"ds_read_b64_tr_b16 v[34:35], v15 offset:12544\n"
"s_waitcnt lgkmcnt(6)\n"
"v_mfma_f32_32x32x16_bf16 v[16:31], v[36:39], v[140:143], v[16:31]\n"
"ds_read_b64_tr_b16 v[36:37], v15 offset:13312\n"
"ds_read_b64_tr_b16 v[38:39], v15 offset:13568\n"
"s_waitcnt lgkmcnt(6)\n"
"v_mfma_f32_32x32x16_bf16 v[16:31], v[40:43], v[144:147], v[16:31]\n"
"ds_read_b64_tr_b16 v[40:41], v15 offset:14336\n"
"ds_read_b64_tr_b16 v[42:43], v15 offset:14592\n"
"s_waitcnt lgkmcnt(6)\n"
"v_mfma_f32_32x32x16_bf16 v[16:31], v[44:47], v[148:151], v[16:31]\n"
"ds_read_b64_tr_b16 v[44:45], v15 offset:15360\n"
"ds_read_b64_tr_b16 v[46:47], v15 offset:15616\n"
"s_waitcnt lgkmcnt(6)\n"
"v_mfma_f32_32x32x16_bf16 v[16:31], v[32:35], v[152:155], v[16:31]\n"
"s_waitcnt lgkmcnt(4)\n"
"v_mfma_f32_32x32x16_bf16 v[16:31], v[36:39], v[156:159], v[16:31]\n"
"s_waitcnt lgkmcnt(2)\n"
"v_mfma_f32_32x32x16_bf16 v[16:31], v[40:43], v[160:163], v[16:31]\n"
"s_waitcnt lgkmcnt(0)\n"
"v_mfma_f32_32x32x16_bf16 v[16:31], v[44:47], v[164:167], v[16:31]\n"
"s_mov_b32 s34, s70\n"
"s_mov_b32 s35, s71\n"
"s_lshl_b32 s70, s58, 3\n"
"s_add_u32 s70, s70, s59\n"
"s_lshl_b32 s70, s70, 4\n"
"s_add_u32 s70, s70, s56\n"
"s_lshl_b32 s70, s70, 7\n"
"s_add_u32 s70, s70, 32768\n"
"s_waitcnt vmcnt(3)\n"
"s_mov_b64 exec, 1\n"
"ds_add_rtn_u32 v54, v52, v53 offset:0\n"
"s_mov_b64 exec, -1\n"
"s_waitcnt lgkmcnt(0)\n"
"v_readfirstlane_b32 s71, v54\n"
"s_nop 1\n"
"s_cmp_lg_u32 s71, 3\n"
"s_cbranch_scc1 L_nf_13_%=\n"
"s_mov_b64 exec, 1\n"
"buffer_store_dwordx2 v[254:255], off, s[52:55], s70 sc1\n"
"s_mov_b64 exec, -1\n"
"s_mov_b32 s1, 1\n"
"L_nf_13_%=:\n"
"s_mov_b32 s70, s34\n"
"s_mov_b32 s71, s35\n"
"v_xor_b32 v69, v82, v65\n"
"v_xor_b32 v77, v82, v73\n"
"v_xor_b32 v70, v82, v66\n"
"v_xor_b32 v78, v82, v74\n"
"v_xor_b32 v71, v82, v67\n"
"v_xor_b32 v79, v82, v75\n"
"s_lshl_b32 s70, s46, 12\n"
"s_add_u32 s70, s70, 78848\n"
"v_add_u32 v60, s70, v0\n"
"ds_write_b128 v60, v[64:67] offset:0\n"
"ds_write_b128 v60, v[68:71] offset:1024\n"
"ds_write_b128 v60, v[72:75] offset:2048\n"
"ds_write_b128 v60, v[76:79] offset:3072\n"
"s_lshl_b32 s70, s47, 5\n"
"s_add_u32 s70, s70, s46\n"
"s_mul_i32 s80, s70, 58368\n"
"s_lshl_b32 s70, s47, 15\n"
"s_lshl_b32 s71, s46, 12\n"
"s_add_u32 s81, s70, s71\n"
"s_add_u32 s70, s83, s46\n"
"s_lshl_b32 s70, s70, 14\n"
"s_lshl_b32 s71, s47, 13\n"
"s_add_u32 s84, s70, s71\n"
"s_add_u32 s84, s84, 0\n"
"s_add_u32 s70, s65, 73728\n"
"s_cmp_lt_u32 s45, 6\n"
"s_cselect_b32 s86, s70, 158720\n"
"s_add_u32 s70, s65, 88064\n"
"s_cmp_lt_u32 s45, 6\n"
"s_cselect_b32 s87, s70, 158720\n"
"s_add_u32 s70, s65, 102400\n"
"s_cmp_lt_u32 s45, 6\n"
"s_cselect_b32 s88, s70, 158720\n"
"s_lshl_b32 s70, s56, 2\n"
"s_add_u32 s70, s70, s46\n"
"s_lshl_b32 s70, s70, 4\n"
"s_add_u32 s70, s70, s57\n"
"s_lshl_b32 s70, s70, 1\n"
"s_add_u32 s70, s70, s47\n"
"s_lshl_b32 s70, s70, 10\n"
"s_add_u32 s89, s70, 8060928\n"
"s_mul_i32 s71, s45, 3584\n"
"s_lshl_b32 s72, s79, 1\n"
"s_add_u32 s82, s71, s72\n"
"s_and_b32 s70, s57, 1\n"
"v_mov_b32 v56, 0x3e8b97f6\n"
"v_xor_b32 v50, s70, v3\n"
"v_cmp_eq_u32 vcc, 1, v50\n"
"s_nop 1\n"
"v_cndmask_b32 v50, v56, 0, vcc\n"
"s_cmp_eq_u32 s70, 1\n"
"s_cselect_b64 vcc, -1, 0\n"
"v_cndmask_b32 v57, v56, v50, vcc\n"
"v_cndmask_b32 v58, v50, v56, vcc\n"
"s_lshr_b32 s70, s46, 1\n"
"s_sub_u32 s70, s70, s46\n"
"s_sub_u32 s70, s70, 2\n"
"s_mul_i32 s70, s70, 58368\n"
"s_and_b32 s71, s46, 1\n"
"s_lshl_b32 s71, s71, 10\n"
"s_add_u32 s70, s70, s71\n"
"v_add_u32 v216, s70, v233\n"
"s_mul_i32 s76, s47, 5120\n"
"s_add_u32 s76, s76, 65536\n"
"v_subrev_u32 v217, 58368, v234\n"
"v_add_u32 v218, s76, v0\n"
"v_lshrrev_b32 v220, 3, v1\n"
"v_and_b32 v223, 7, v1\n"
"v_xor_b32 v219, v223, v220\n"
"v_lshlrev_b32 v219, 4, v219\n"
"v_lshl_add_u32 v219, v220, 7, v219\n"
"s_add_u32 s70, s65, 115712\n"
"v_add_u32 v219, s70, v219\n"
"v_lshlrev_b32 v220, 16, v220\n"
"v_lshl_add_u32 v220, v223, 4, v220\n"
"s_lshl_b32 s70, s46, 19\n"
"s_lshl_b32 s71, s62, 1\n"
"s_add_u32 s71, s71, s47\n"
"s_lshl_b32 s71, s71, 13\n"
"s_add_u32 s70, s70, s71\n"
"s_lshl_b32 s71, s61, 7\n"
"s_add_u32 s70, s70, s71\n"
"s_add_u32 s33, s70, 3866624\n"
"s_cmp_lt_u32 s45, 6\n"
"s_cselect_b32 s70, 28672, 0x40000000\n"
"v_add_u32 v253, s70, v240\n"
"s_cmp_eq_u32 s47, 1\n"
"s_cselect_b32 s48, s40, s36\n"
"s_cselect_b32 s49, s41, s37\n"
"s_mov_b32 s50, s38\n"
"s_mov_b32 s51, s39\n"
"v_mov_b32 v4, -1\n"
"v_mov_b32 v5, -1\n"
"v_mov_b32 v6, -1\n"
"v_mov_b32 v7, -1\n"
"s_lshl_b32 s75, s46, 10\n"
"s_cmp_eq_u32 s1, 1\n"
"s_cbranch_scc1 L_wq1_14_%=\n"
"s_waitcnt vmcnt(0)\n"
"s_branch L_wq2_15_%=\n"
"L_wq1_14_%=:\n"
"s_waitcnt vmcnt(1)\n"
"L_wq2_15_%=:\n"
"s_mov_b64 exec, 1\n"
"ds_add_u32 v52, v53 offset:16\n"
"s_mov_b64 exec, -1\n"
"L_sp_16_%=:\n"
"ds_read_b32 v54, v52 offset:16\n"
"s_waitcnt lgkmcnt(0)\n"
"v_readfirstlane_b32 s71, v54\n"
"s_nop 1\n"
"s_cmp_ge_u32 s71, 4\n"
"s_cbranch_scc1 L_sd_17_%=\n"
"s_cmp_eq_u32 s85, 0\n"
"s_cbranch_scc1 L_sd_17_%=\n"
"s_sub_u32 s85, s85, 1\n"
"s_branch L_sp_16_%=\n"
"L_sd_17_%=:\n"
"ds_read_b64_tr_b16 v[32:33], v15 offset:16384\n"
"ds_read_b64_tr_b16 v[34:35], v15 offset:16640\n"
"ds_read_b64_tr_b16 v[36:37], v15 offset:17408\n"
"ds_read_b64_tr_b16 v[38:39], v15 offset:17664\n"
"ds_read_b64_tr_b16 v[40:41], v15 offset:18432\n"
"ds_read_b64_tr_b16 v[42:43], v15 offset:18688\n"
"ds_read_b64_tr_b16 v[44:45], v15 offset:19456\n"
"ds_read_b64_tr_b16 v[46:47], v15 offset:19712\n"
"s_waitcnt lgkmcnt(6)\n"
"v_mfma_f32_32x32x16_bf16 v[16:31], v[32:35], v[168:171], v[16:31]\n"
"ds_read_b64_tr_b16 v[32:33], v15 offset:20480\n"
"ds_read_b64_tr_b16 v[34:35], v15 offset:20736\n"
"s_waitcnt lgkmcnt(6)\n"
"v_mfma_f32_32x32x16_bf16 v[16:31], v[36:39], v[172:175], v[16:31]\n"
"ds_read_b64_tr_b16 v[36:37], v15 offset:21504\n"
"ds_read_b64_tr_b16 v[38:39], v15 offset:21760\n"
"s_waitcnt lgkmcnt(6)\n"
"v_mfma_f32_32x32x16_bf16 v[16:31], v[40:43], v[176:179], v[16:31]\n"
"ds_read_b64_tr_b16 v[40:41], v15 offset:22528\n"
"ds_read_b64_tr_b16 v[42:43], v15 offset:22784\n"
"s_waitcnt lgkmcnt(6)\n"
"v_mfma_f32_32x32x16_bf16 v[16:31], v[44:47], v[180:183], v[16:31]\n"
"ds_read_b64_tr_b16 v[44:45], v15 offset:23552\n"
"ds_read_b64_tr_b16 v[46:47], v15 offset:23808\n"
"s_waitcnt lgkmcnt(6)\n"
"v_mfma_f32_32x32x16_bf16 v[16:31], v[32:35], v[184:187], v[16:31]\n"
"ds_read_b64_tr_b16 v[32:33], v15 offset:24576\n"
"ds_read_b64_tr_b16 v[34:35], v15 offset:24832\n"
"s_waitcnt lgkmcnt(6)\n"
"v_mfma_f32_32x32x16_bf16 v[16:31], v[36:39], v[188:191], v[16:31]\n"
"ds_read_b64_tr_b16 v[36:37], v15 offset:25600\n"
"ds_read_b64_tr_b16 v[38:39], v15 offset:25856\n"
"s_waitcnt lgkmcnt(6)\n"
"v_mfma_f32_32x32x16_bf16 v[16:31], v[40:43], v[192:195], v[16:31]\n"
"ds_read_b64_tr_b16 v[40:41], v15 offset:26624\n"
"ds_read_b64_tr_b16 v[42:43], v15 offset:26880\n"
"s_waitcnt lgkmcnt(6)\n"
"v_mfma_f32_32x32x16_bf16 v[16:31], v[44:47], v[196:199], v[16:31]\n"
"ds_read_b64_tr_b16 v[44:45], v15 offset:27648\n"
"ds_read_b64_tr_b16 v[46:47], v15 offset:27904\n"
"s_cmp_lg_u32 s45, 0\n"
"s_cbranch_scc1 L_ep_18_%=\n"
"buffer_load_dwordx2 v[54:55], v59, s[12:15], 0 offen sc1\n"
"L_ep_18_%=:\n"
"s_waitcnt lgkmcnt(6)\n"
"v_mfma_f32_32x32x16_bf16 v[16:31], v[32:35], v[200:203], v[16:31]\n"
"s_waitcnt lgkmcnt(4)\n"
"v_mfma_f32_32x32x16_bf16 v[16:31], v[36:39], v[204:207], v[16:31]\n"
"s_waitcnt lgkmcnt(2)\n"
"v_mfma_f32_32x32x16_bf16 v[16:31], v[40:43], v[208:211], v[16:31]\n"
"s_waitcnt lgkmcnt(0)\n"
"v_mfma_f32_32x32x16_bf16 v[16:31], v[44:47], v[212:215], v[16:31]\n"
"s_nop 11\n"
"v_mul_f32 v16, v16, v57\n"
"v_mul_f32 v17, v17, v57\n"
"v_mul_f32 v18, v18, v57\n"
"v_mul_f32 v19, v19, v57\n"
"v_mul_f32 v20, v20, v56\n"
"v_mul_f32 v21, v21, v56\n"
"v_mul_f32 v22, v22, v56\n"
"v_mul_f32 v23, v23, v56\n"
"v_mul_f32 v24, v24, v56\n"
"v_mul_f32 v25, v25, v56\n"
"v_mul_f32 v26, v26, v56\n"
"v_mul_f32 v27, v27, v56\n"
"v_mul_f32 v28, v28, v58\n"
"v_mul_f32 v29, v29, v58\n"
"v_mul_f32 v30, v30, v58\n"
"v_mul_f32 v31, v31, v58\n"
"v_cvt_pk_bf16_f32 v8, v16, v17\n"
"v_cvt_pk_bf16_f32 v9, v18, v19\n"
"v_cvt_pk_bf16_f32 v10, v20, v21\n"
"v_cvt_pk_bf16_f32 v11, v22, v23\n"
"v_cvt_pk_bf16_f32 v12, v24, v25\n"
"v_cvt_pk_bf16_f32 v13, v26, v27\n"
"v_cvt_pk_bf16_f32 v14, v28, v29\n"
"v_cvt_pk_bf16_f32 v15, v30, v31\n"
"s_lshl_b32 s70, s46, 11\n"
"s_add_u32 s70, s70, 99328\n"
"v_add_u32 v42, s70, v0\n"
"ds_write_b128 v42, v[8:11]\n"
"ds_write_b128 v42, v[12:15] offset:1024\n"
"ds_read_b128 v[136:139], v60 offset:0\n"
"ds_read_b128 v[140:143], v60 offset:1024\n"
"ds_read_b128 v[144:147], v60 offset:2048\n"
"ds_read_b128 v[148:151], v60 offset:3072\n"
"v_mov_b32 v160, 0\n"
"v_mov_b32 v161, 0\n"
"v_mov_b32 v162, 0\n"
"v_mov_b32 v163, 0\n"
"v_mov_b32 v164, 0\n"
"v_mov_b32 v165, 0\n"
"v_mov_b32 v166, 0\n"
"v_mov_b32 v167, 0\n"
"s_cmp_lg_u32 s45, 0\n"
"s_cbranch_scc1 L_nopoll_19_%=\n"
"buffer_load_dwordx2 v[64:65], v59, s[12:15], 0 offen sc1\n"
"s_sleep 2\n"
"buffer_load_dwordx2 v[66:67], v59, s[12:15], 0 offen sc1\n"
"s_sleep 2\n"
"L_kvr_20_%=:\n"
"s_waitcnt vmcnt(2)\n"
"v_cmp_ne_u64 vcc, s[66:67], v[54:55]\n"
"s_nop 4\n"
"s_cbranch_vccz L_nopoll_19_%=\n"
"buffer_load_dwordx2 v[54:55], v59, s[12:15], 0 offen sc1\n"
"s_waitcnt vmcnt(2)\n"
"v_cmp_ne_u64 vcc, s[66:67], v[64:65]\n"
"s_nop 4\n"
"s_cbranch_vccz L_nopoll_19_%=\n"
"buffer_load_dwordx2 v[64:65], v59, s[12:15], 0 offen sc1\n"
"s_waitcnt vmcnt(2)\n"
"v_cmp_ne_u64 vcc, s[66:67], v[66:67]\n"
"s_nop 4\n"
"s_cbranch_vccz L_nopoll_19_%=\n"
"buffer_load_dwordx2 v[66:67], v59, s[12:15], 0 offen sc1\n"
"s_cmp_eq_u32 s85, 0\n"
"s_cbranch_scc1 L_nopoll_19_%=\n"
"s_sub_u32 s85, s85, 1\n"
"s_sleep 1\n"
"s_branch L_kvr_20_%=\n"
"L_nopoll_19_%=:\n"
"s_waitcnt lgkmcnt(0)\n"
"L_j2_4_%=:\n"
"s_barrier\n"
"s_add_u32 m0, s76, s75\n"
"s_cmp_lg_u32 s46, 0\n"
"buffer_load_dwordx4 v216, s[12:15], s80 offen sc1 lds\n"
"s_cbranch_scc1 L_nov_27_%=\n"
"s_add_u32 m0, s76, 2048\n"
"s_nop 0\n"
"buffer_load_dwordx4 v217, s[12:15], s80 offen offset:2048 sc1 lds\n"
"L_nov_27_%=:\n"
"s_barrier\n"
"s_cmp_eq_u32 s47, 0\n"
"s_cbranch_scc1 L_b1s2_28_%=\n"
"ds_read_b128 v[88:91], v43\n"
"ds_read_b128 v[92:95], v43 offset:1024\n"
"ds_read_b128 v[8:11], v44\n"
"ds_read_b128 v[12:15], v44 offset:1024\n"
"ds_read_b128 v[136:139], v45 offset:0\n"
"ds_read_b128 v[140:143], v45 offset:1024\n"
"ds_read_b128 v[144:147], v45 offset:2048\n"
"ds_read_b128 v[148:151], v45 offset:3072\n"
"v_mfma_f32_32x32x16_bf16 v[16:31], v[80:83], v[64:67], 0\n"
"v_mfma_f32_32x32x16_bf16 v[16:31], v[84:87], v[68:71], v[16:31]\n"
"s_waitcnt lgkmcnt(6)\n"
"v_mfma_f32_32x32x16_bf16 v[16:31], v[88:91], v[72:75], v[16:31]\n"
"v_mfma_f32_32x32x16_bf16 v[16:31], v[92:95], v[76:79], v[16:31]\n"
"s_nop 11\n"
"v_cvt_pk_bf16_f32 v80, v16, v17\n"
"v_cvt_pk_bf16_f32 v81, v18, v19\n"
"v_cvt_pk_bf16_f32 v82, v20, v21\n"
"v_cvt_pk_bf16_f32 v83, v22, v23\n"
"v_cvt_pk_bf16_f32 v84, v24, v25\n"
"v_cvt_pk_bf16_f32 v85, v26, v27\n"
"v_cvt_pk_bf16_f32 v86, v28, v29\n"
"v_cvt_pk_bf16_f32 v87, v30, v31\n"
"ds_write_b128 v221, v[80:83]\n"
"ds_write_b128 v222, v[84:87]\n"
"s_add_u32 m0, s81, 0\n"
"s_nop 0\n"
"buffer_load_dwordx4 v233, s[12:15], s80 offen sc1 lds\n"
"buffer_load_dwordx4 v233, s[12:15], s80 offen offset:1024 sc1 lds\n"
"buffer_load_dwordx4 v234, s[12:15], s80 offen offset:2048 sc1 lds\n"
"buffer_load_dwordx4 v235, s[12:15], s80 offen offset:3072 sc1 lds\n"
"s_add_u32 s80, s80, 233472\n"
"s_branch L_b1j_29_%=\n"
"L_b1s2_28_%=:\n"
"s_add_u32 m0, s81, 0\n"
"s_nop 0\n"
"buffer_load_dwordx4 v233, s[12:15], s80 offen sc1 lds\n"
"buffer_load_dwordx4 v233, s[12:15], s80 offen offset:1024 sc1 lds\n"
"buffer_load_dwordx4 v234, s[12:15], s80 offen offset:2048 sc1 lds\n"
"buffer_load_dwordx4 v235, s[12:15], s80 offen offset:3072 sc1 lds\n"
"s_add_u32 s80, s80, 233472\n"
"L_b1j_29_%=:\n"
"s_waitcnt vmcnt(4) lgkmcnt(0)\n"
"s_barrier\n"
"ds_read_b128 v[80:83], v218\n"
"ds_read_b128 v[84:87], v218 offset:1024\n"
"ds_read_b128 v[100:103], v219\n"
"ds_read_b128 v[88:91], v218 offset:4096\n"
"s_waitcnt lgkmcnt(2)\n"
"v_mfma_f32_32x32x16_bf16 v[32:47], v[80:83], v[8:11], 0\n"
"v_mfma_f32_32x32x16_bf16 v[32:47], v[84:87], v[12:15], v[32:47]\n"
"ds_read_b128 v[80:83], v218 offset:2048\n"
"ds_read_b128 v[84:87], v218 offset:3072\n"
"s_add_u32 m0, s81, 16384\n"
"s_nop 0\n"
"buffer_load_dwordx4 v233, s[12:15], s80 offen sc1 lds\n"
"buffer_load_dwordx4 v233, s[12:15], s80 offen offset:1024 sc1 lds\n"
"buffer_load_dwordx4 v234, s[12:15], s80 offen offset:2048 sc1 lds\n"
"buffer_load_dwordx4 v235, s[12:15], s80 offen offset:3072 sc1 lds\n"
"s_add_u32 s80, s80, 233472\n"
"s_nop 2\n"
"v_max3_f32 v108, v32, v33, v34\n"
"v_max3_f32 v109, v35, v36, v37\n"
"v_max3_f32 v108, v108, v38, v39\n"
"v_max3_f32 v109, v109, v40, v41\n"
"v_max3_f32 v108, v108, v42, v43\n"
"v_max3_f32 v109, v109, v44, v45\n"
"v_max3_f32 v108, v108, v46, v47\n"
"v_max_f32 v108, v108, v109\n"
"v_mov_b32 v109, v108\n"
"s_waitcnt lgkmcnt(3)\n"
"buffer_store_dwordx4 v[100:103], v220, s[12:15], s33 offen sc1\n"
"v_permlane32_swap_b32 v108, v109\n"
"v_max_f32 v226, v108, v109\n"
"v_pk_add_f32 v[32:33], v[32:33], v[226:227] op_sel_hi:[1,0] neg_lo:[0,1] neg_hi:[0,1]\n"
"v_pk_add_f32 v[34:35], v[34:35], v[226:227] op_sel_hi:[1,0] neg_lo:[0,1] neg_hi:[0,1]\n"
"v_pk_add_f32 v[36:37], v[36:37], v[226:227] op_sel_hi:[1,0] neg_lo:[0,1] neg_hi:[0,1]\n"
"v_pk_add_f32 v[38:39], v[38:39], v[226:227] op_sel_hi:[1,0] neg_lo:[0,1] neg_hi:[0,1]\n"
"v_pk_add_f32 v[40:41], v[40:41], v[226:227] op_sel_hi:[1,0] neg_lo:[0,1] neg_hi:[0,1]\n"
"v_pk_add_f32 v[42:43], v[42:43], v[226:227] op_sel_hi:[1,0] neg_lo:[0,1] neg_hi:[0,1]\n"
"v_pk_add_f32 v[44:45], v[44:45], v[226:227] op_sel_hi:[1,0] neg_lo:[0,1] neg_hi:[0,1]\n"
"v_pk_add_f32 v[46:47], v[46:47], v[226:227] op_sel_hi:[1,0] neg_lo:[0,1] neg_hi:[0,1]\n"
"v_exp_f32 v32, v32\n"
"v_exp_f32 v33, v33\n"
"v_exp_f32 v34, v34\n"
"v_exp_f32 v35, v35\n"
"v_exp_f32 v36, v36\n"
"v_exp_f32 v37, v37\n"
"v_exp_f32 v38, v38\n"
"v_exp_f32 v39, v39\n"
"v_pk_add_f32 v[16:17], v[226:227], 0 op_sel_hi:[0,0] neg_lo:[1,0] neg_hi:[1,0]\n"
"v_pk_add_f32 v[18:19], v[226:227], 0 op_sel_hi:[0,0] neg_lo:[1,0] neg_hi:[1,0]\n"
"v_pk_add_f32 v[20:21], v[226:227], 0 op_sel_hi:[0,0] neg_lo:[1,0] neg_hi:[1,0]\n"
"v_pk_add_f32 v[22:23], v[226:227], 0 op_sel_hi:[0,0] neg_lo:[1,0] neg_hi:[1,0]\n"
"v_pk_add_f32 v[24:25], v[226:227], 0 op_sel_hi:[0,0] neg_lo:[1,0] neg_hi:[1,0]\n"
"v_pk_add_f32 v[26:27], v[226:227], 0 op_sel_hi:[0,0] neg_lo:[1,0] neg_hi:[1,0]\n"
"v_pk_add_f32 v[28:29], v[226:227], 0 op_sel_hi:[0,0] neg_lo:[1,0] neg_hi:[1,0]\n"
"v_pk_add_f32 v[30:31], v[226:227], 0 op_sel_hi:[0,0] neg_lo:[1,0] neg_hi:[1,0]\n"
"s_waitcnt lgkmcnt(0)\n"
"s_waitcnt vmcnt(5)\n"
"s_barrier\n"
"v_mfma_f32_32x32x16_bf16 v[48:63], v[80:83], v[8:11], v[16:31]\n"
"s_setprio 2\n"
"ds_read_b128 v[80:83], v232 offset:0\n"
"v_cvt_pk_bf16_f32 v96, v32, v33\n"
"v_cvt_pk_bf16_f32 v97, v34, v35\n"
"v_exp_f32 v40, v40\n"
"v_exp_f32 v41, v41\n"
"buffer_store_dwordx4 v[4:7], v0, s[12:15], s89 offen sc1\n"
"v_mfma_f32_32x32x16_bf16 v[48:63], v[84:87], v[12:15], v[48:63]\n"
"ds_read_b128 v[84:87], v232 offset:1024\n"
"v_cvt_pk_bf16_f32 v98, v36, v37\n"
"v_cvt_pk_bf16_f32 v99, v38, v39\n"
"v_exp_f32 v42, v42\n"
"v_exp_f32 v43, v43\n"
"ds_read_b128 v[92:95], v232 offset:3072\n"
"v_exp_f32 v44, v44\n"
"v_exp_f32 v45, v45\n"
"v_cvt_pk_bf16_f32 v100, v40, v41\n"
"v_cvt_pk_bf16_f32 v101, v42, v43\n"
"v_mfma_f32_32x32x16_bf16 v[64:79], v[88:91], v[96:99], 0\n"
"ds_read_b128 v[88:91], v232 offset:2048\n"
"v_exp_f32 v46, v46\n"
"v_exp_f32 v47, v47\n"
"v_cvt_pk_bf16_f32 v102, v44, v45\n"
"v_cvt_pk_bf16_f32 v103, v46, v47\n"
"s_setprio 0\n"
"v_exp_f32 v48, v48\n"
"v_exp_f32 v49, v49\n"
"v_exp_f32 v50, v50\n"
"v_exp_f32 v51, v51\n"
"v_exp_f32 v52, v52\n"
"v_exp_f32 v53, v53\n"
"v_exp_f32 v54, v54\n"
"v_exp_f32 v55, v55\n"
"s_waitcnt lgkmcnt(0)\n"
"v_mfma_f32_32x32x16_bf16 v[32:47], v[80:83], v[8:11], v[16:31]\n"
"s_setprio 2\n"
"ds_read_b128 v[80:83], v232 offset:4096\n"
"v_cvt_pk_bf16_f32 v96, v48, v49\n"
"v_cvt_pk_bf16_f32 v97, v50, v51\n"
"v_exp_f32 v56, v56\n"
"v_exp_f32 v57, v57\n"
"s_mul_i32 s71, s63, 225\n"
"s_mul_i32 s72, s64, 29\n"
"s_add_u32 s71, s71, s72\n"
"s_and_b32 s71, s71, -2\n"
"s_lshl_b32 s71, s71, 1\n"
"s_mul_i32 s72, s46, 230400\n"
"s_add_u32 s73, s71, s72\n"
"v_lshrrev_b32 v225, 2, v1\n"
"v_mul_u32_u24 v225, 900, v225\n"
"v_and_or_b32 v224, v1, 3, 0\n"
"v_add_lshl_u32 v225, v225, v224, 4\n"
"v_mfma_f32_32x32x16_bf16 v[32:47], v[84:87], v[12:15], v[32:47]\n"
"ds_read_b128 v[84:87], v232 offset:5120\n"
"v_cvt_pk_bf16_f32 v98, v52, v53\n"
"v_cvt_pk_bf16_f32 v99, v54, v55\n"
"v_exp_f32 v58, v58\n"
"v_exp_f32 v59, v59\n"
"v_mfma_f32_32x32x16_bf16 v[64:79], v[92:95], v[100:103], v[64:79]\n"
"ds_read_b128 v[92:95], v232 offset:7168\n"
"v_exp_f32 v60, v60\n"
"v_exp_f32 v61, v61\n"
"v_cvt_pk_bf16_f32 v100, v56, v57\n"
"v_cvt_pk_bf16_f32 v101, v58, v59\n"
"v_mfma_f32_32x32x16_bf16 v[64:79], v[88:91], v[96:99], v[64:79]\n"
"ds_read_b128 v[88:91], v232 offset:6144\n"
"v_exp_f32 v62, v62\n"
"v_exp_f32 v63, v63\n"
"v_cvt_pk_bf16_f32 v102, v60, v61\n"
"v_cvt_pk_bf16_f32 v103, v62, v63\n"
"s_setprio 0\n"
"v_exp_f32 v32, v32\n"
"v_exp_f32 v33, v33\n"
"v_exp_f32 v34, v34\n"
"v_exp_f32 v35, v35\n"
"v_exp_f32 v36, v36\n"
"v_exp_f32 v37, v37\n"
"v_exp_f32 v38, v38\n"
"v_exp_f32 v39, v39\n"
"s_waitcnt lgkmcnt(0)\n"
"v_mfma_f32_32x32x16_bf16 v[48:63], v[80:83], v[8:11], v[16:31]\n"
"s_setprio 2\n"
"ds_read_b128 v[80:83], v232 offset:8192\n"
"v_cvt_pk_bf16_f32 v96, v32, v33\n"
"v_cvt_pk_bf16_f32 v97, v34, v35\n"
"v_exp_f32 v40, v40\n"
"v_exp_f32 v41, v41\n"
"v_xor_b32 v152, 0x80008000, v144\n"
"v_xor_b32 v153, 0x80008000, v145\n"
"v_mfma_f32_32x32x16_bf16 v[48:63], v[84:87], v[12:15], v[48:63]\n"
"ds_read_b128 v[84:87], v232 offset:9216\n"
"v_cvt_pk_bf16_f32 v98, v36, v37\n"
"v_cvt_pk_bf16_f32 v99, v38, v39\n"
"v_exp_f32 v42, v42\n"
"v_exp_f32 v43, v43\n"
"v_xor_b32 v154, 0x80008000, v146\n"
"v_xor_b32 v155, 0x80008000, v147\n"
"v_mfma_f32_32x32x16_bf16 v[64:79], v[92:95], v[100:103], v[64:79]\n"
"ds_read_b128 v[92:95], v232 offset:11264\n"
"v_exp_f32 v44, v44\n"
"v_exp_f32 v45, v45\n"
"v_cvt_pk_bf16_f32 v100, v40, v41\n"
"v_cvt_pk_bf16_f32 v101, v42, v43\n"
"v_xor_b32 v156, 0x80008000, v148\n"
"v_xor_b32 v157, 0x80008000, v149\n"
"v_mfma_f32_32x32x16_bf16 v[64:79], v[88:91], v[96:99], v[64:79]\n"
"ds_read_b128 v[88:91], v232 offset:10240\n"
"v_exp_f32 v46, v46\n"
"v_exp_f32 v47, v47\n"
"v_cvt_pk_bf16_f32 v102, v44, v45\n"
"v_cvt_pk_bf16_f32 v103, v46, v47\n"
"v_xor_b32 v158, 0x80008000, v150\n"
"v_xor_b32 v159, 0x80008000, v151\n"
"s_setprio 0\n"
"v_exp_f32 v48, v48\n"
"v_exp_f32 v49, v49\n"
"v_exp_f32 v50, v50\n"
"v_exp_f32 v51, v51\n"
"v_exp_f32 v52, v52\n"
"v_exp_f32 v53, v53\n"
"v_exp_f32 v54, v54\n"
"v_exp_f32 v55, v55\n"
"s_waitcnt vmcnt(1) lgkmcnt(0)\n"
"s_barrier\n"
"v_mfma_f32_32x32x16_bf16 v[32:47], v[80:83], v[8:11], v[16:31]\n"
"s_setprio 2\n"
"ds_read_b128 v[80:83], v232 offset:12288\n"
"v_cvt_pk_bf16_f32 v96, v48, v49\n"
"v_cvt_pk_bf16_f32 v97, v50, v51\n"
"v_exp_f32 v56, v56\n"
"v_exp_f32 v57, v57\n"
"s_cmp_lg_u32 s45, 7\n"
"s_cbranch_scc1 L_pf_30_%=\n"
"s_lshl_b32 s70, s62, 6\n"
"s_add_u32 s70, s70, s61\n"
"s_lshl_b32 s70, s70, 7\n"
"s_add_u32 s70, s70, 65536\n"
"s_mov_b64 exec, 1\n"
"buffer_store_dwordx2 v[254:255], off, s[12:15], s70 sc1\n"
"s_mov_b64 exec, -1\n"
"L_pf_30_%=:\n"
"v_mfma_f32_32x32x16_bf16 v[32:47], v[84:87], v[12:15], v[32:47]\n"
"ds_read_b128 v[84:87], v232 offset:13312\n"
"v_cvt_pk_bf16_f32 v98, v52, v53\n"
"v_cvt_pk_bf16_f32 v99, v54, v55\n"
"v_exp_f32 v58, v58\n"
"v_exp_f32 v59, v59\n"
"s_mov_b32 m0, s86\n"
"s_nop 0\n"
"buffer_load_dwordx4 v253, s[24:27], s82 offen lds\n"
"s_add_u32 m0, s65, 65536\n"
"v_mfma_f32_32x32x16_bf16 v[64:79], v[92:95], v[100:103], v[64:79]\n"
"ds_read_b128 v[92:95], v232 offset:15360\n"
"v_exp_f32 v60, v60\n"
"v_exp_f32 v61, v61\n"
"v_cvt_pk_bf16_f32 v100, v56, v57\n"
"v_cvt_pk_bf16_f32 v101, v58, v59\n"
"v_mfma_f32_32x32x16_bf16 v[64:79], v[88:91], v[96:99], v[64:79]\n"
"ds_read_b128 v[88:91], v232 offset:14336\n"
"v_exp_f32 v62, v62\n"
"v_exp_f32 v63, v63\n"
"v_cvt_pk_bf16_f32 v102, v60, v61\n"
"v_cvt_pk_bf16_f32 v103, v62, v63\n"
"buffer_load_dwordx4 v240, s[24:27], s82 offen lds\n"
"s_add_u32 s82, s82, 50176\n"
"s_setprio 0\n"
"v_exp_f32 v32, v32\n"
"v_exp_f32 v33, v33\n"
"v_exp_f32 v34, v34\n"
"v_exp_f32 v35, v35\n"
"v_exp_f32 v36, v36\n"
"v_exp_f32 v37, v37\n"
"v_exp_f32 v38, v38\n"
"v_exp_f32 v39, v39\n"
"s_waitcnt lgkmcnt(0)\n"
"s_barrier\n"
"v_mfma_f32_32x32x16_bf16 v[48:63], v[80:83], v[8:11], v[16:31]\n"
"s_setprio 2\n"
"ds_read_b128 v[80:83], v232 offset:16384\n"
"v_cvt_pk_bf16_f32 v96, v32, v33\n"
"v_cvt_pk_bf16_f32 v97, v34, v35\n"
"v_exp_f32 v40, v40\n"
"v_exp_f32 v41, v41\n"
"s_add_u32 m0, s81, 0\n"
"s_nop 0\n"
"buffer_load_dwordx4 v233, s[12:15], s80 offen sc1 lds\n"
"v_mfma_f32_32x32x16_bf16 v[48:63], v[84:87], v[12:15], v[48:63]\n"
"ds_read_b128 v[84:87], v232 offset:17408\n"
"v_cvt_pk_bf16_f32 v98, v36, v37\n"
"v_cvt_pk_bf16_f32 v99, v38, v39\n"
"v_exp_f32 v42, v42\n"
"v_exp_f32 v43, v43\n"
"v_mfma_f32_32x32x16_bf16 v[64:79], v[92:95], v[100:103], v[64:79]\n"
"ds_read_b128 v[92:95], v232 offset:19456\n"
"v_exp_f32 v44, v44\n"
"v_exp_f32 v45, v45\n"
"v_cvt_pk_bf16_f32 v100, v40, v41\n"
"v_cvt_pk_bf16_f32 v101, v42, v43\n"
"buffer_load_dwordx4 v233, s[12:15], s80 offen offset:1024 sc1 lds\n"
"v_mfma_f32_32x32x16_bf16 v[64:79], v[88:91], v[96:99], v[64:79]\n"
"ds_read_b128 v[88:91], v232 offset:18432\n"
"v_exp_f32 v46, v46\n"
"v_exp_f32 v47, v47\n"
"v_cvt_pk_bf16_f32 v102, v44, v45\n"
"v_cvt_pk_bf16_f32 v103, v46, v47\n"
"s_setprio 0\n"
"v_exp_f32 v48, v48\n"
"v_exp_f32 v49, v49\n"
"v_exp_f32 v50, v50\n"
"v_exp_f32 v51, v51\n"
"v_exp_f32 v52, v52\n"
"v_exp_f32 v53, v53\n"
"v_exp_f32 v54, v54\n"
"v_exp_f32 v55, v55\n"
"s_waitcnt lgkmcnt(0)\n"
"v_mfma_f32_32x32x16_bf16 v[32:47], v[80:83], v[8:11], v[16:31]\n"
"s_setprio 2\n"
"ds_read_b128 v[80:83], v232 offset:20480\n"
"v_cvt_pk_bf16_f32 v96, v48, v49\n"
"v_cvt_pk_bf16_f32 v97, v50, v51\n"
"v_exp_f32 v56, v56\n"
"v_exp_f32 v57, v57\n"
"buffer_load_dwordx4 v234, s[12:15], s80 offen offset:2048 sc1 lds\n"
"v_mfma_f32_32x32x16_bf16 v[32:47], v[84:87], v[12:15], v[32:47]\n"
"ds_read_b128 v[84:87], v232 offset:21504\n"
"v_cvt_pk_bf16_f32 v98, v52, v53\n"
"v_cvt_pk_bf16_f32 v99, v54, v55\n"
"v_exp_f32 v58, v58\n"
"v_exp_f32 v59, v59\n"
"v_mfma_f32_32x32x16_bf16 v[64:79], v[92:95], v[100:103], v[64:79]\n"
"ds_read_b128 v[92:95], v232 offset:23552\n"
"v_exp_f32 v60, v60\n"
"v_exp_f32 v61, v61\n"
"v_cvt_pk_bf16_f32 v100, v56, v57\n"
"v_cvt_pk_bf16_f32 v101, v58, v59\n"
"buffer_load_dwordx4 v235, s[12:15], s80 offen offset:3072 sc1 lds\n"
"s_add_u32 s80, s80, 233472\n"
"v_mfma_f32_32x32x16_bf16 v[64:79], v[88:91], v[96:99], v[64:79]\n"
"ds_read_b128 v[88:91], v232 offset:22528\n"
"v_exp_f32 v62, v62\n"
"v_exp_f32 v63, v63\n"
"v_cvt_pk_bf16_f32 v102, v60, v61\n"
"v_cvt_pk_bf16_f32 v103, v62, v63\n"
"s_mov_b32 m0, s87\n"
"s_setprio 0\n"
"v_exp_f32 v32, v32\n"
"v_exp_f32 v33, v33\n"
"v_exp_f32 v34, v34\n"
"v_exp_f32 v35, v35\n"
"v_exp_f32 v36, v36\n"
"v_exp_f32 v37, v37\n"
"v_exp_f32 v38, v38\n"
"v_exp_f32 v39, v39\n"
"s_waitcnt lgkmcnt(0)\n"
"v_mfma_f32_32x32x16_bf16 v[48:63], v[80:83], v[8:11], v[16:31]\n"
"s_setprio 2\n"
"ds_read_b128 v[80:83], v232 offset:24576\n"
"v_cvt_pk_bf16_f32 v96, v32, v33\n"
"v_cvt_pk_bf16_f32 v97, v34, v35\n"
"v_exp_f32 v40, v40\n"
"v_exp_f32 v41, v41\n"
"buffer_load_dwordx4 v253, s[24:27], s82 offen lds\n"
"s_add_u32 m0, s65, 79872\n"
"v_mfma_f32_32x32x16_bf16 v[48:63], v[84:87], v[12:15], v[48:63]\n"
"ds_read_b128 v[84:87], v232 offset:25600\n"
"v_cvt_pk_bf16_f32 v98, v36, v37\n"
"v_cvt_pk_bf16_f32 v99, v38, v39\n"
"v_exp_f32 v42, v42\n"
"v_exp_f32 v43, v43\n"
"v_mfma_f32_32x32x16_bf16 v[64:79], v[92:95], v[100:103], v[64:79]\n"
"ds_read_b128 v[92:95], v232 offset:27648\n"
"v_exp_f32 v44, v44\n"
"v_exp_f32 v45, v45\n"
"v_cvt_pk_bf16_f32 v100, v40, v41\n"
"v_cvt_pk_bf16_f32 v101, v42, v43\n"
"buffer_load_dwordx4 v240, s[24:27], s82 offen lds\n"
"s_add_u32 s82, s82, 50176\n"
"v_mfma_f32_32x32x16_bf16 v[64:79], v[88:91], v[96:99], v[64:79]\n"
"ds_read_b128 v[88:91], v232 offset:26624\n"
"v_exp_f32 v46, v46\n"
"v_exp_f32 v47, v47\n"
"v_cvt_pk_bf16_f32 v102, v44, v45\n"
"v_cvt_pk_bf16_f32 v103, v46, v47\n"
"s_setprio 0\n"
"v_exp_f32 v48, v48\n"
"v_exp_f32 v49, v49\n"
"v_exp_f32 v50, v50\n"
"v_exp_f32 v51, v51\n"
"v_exp_f32 v52, v52\n"
"v_exp_f32 v53, v53\n"
"v_exp_f32 v54, v54\n"
"v_exp_f32 v55, v55\n"
"s_waitcnt lgkmcnt(0)\n"
"v_mfma_f32_32x32x16_bf16 v[32:47], v[80:83], v[8:11], v[16:31]\n"
"s_setprio 2\n"
"ds_read_b128 v[80:83], v232 offset:28672\n"
"v_cvt_pk_bf16_f32 v96, v48, v49\n"
"v_cvt_pk_bf16_f32 v97, v50, v51\n"
"v_exp_f32 v56, v56\n"
"v_exp_f32 v57, v57\n"
"buffer_load_dwordx2 v[4:5], v250, s[12:15], 0 offen sc1\n"
"v_mfma_f32_32x32x16_bf16 v[32:47], v[84:87], v[12:15], v[32:47]\n"
"ds_read_b128 v[84:87], v232 offset:29696\n"
"v_cvt_pk_bf16_f32 v98, v52, v53\n"
"v_cvt_pk_bf16_f32 v99, v54, v55\n"
"v_exp_f32 v58, v58\n"
"v_exp_f32 v59, v59\n"
"v_mfma_f32_32x32x16_bf16 v[64:79], v[92:95], v[100:103], v[64:79]\n"
"ds_read_b128 v[92:95], v232 offset:31744\n"
"v_exp_f32 v60, v60\n"
"v_exp_f32 v61, v61\n"
"v_cvt_pk_bf16_f32 v100, v56, v57\n"
"v_cvt_pk_bf16_f32 v101, v58, v59\n"
"v_mfma_f32_32x32x16_bf16 v[64:79], v[88:91], v[96:99], v[64:79]\n"
"ds_read_b128 v[88:91], v232 offset:30720\n"
"v_exp_f32 v62, v62\n"
"v_exp_f32 v63, v63\n"
"v_cvt_pk_bf16_f32 v102, v60, v61\n"
"v_cvt_pk_bf16_f32 v103, v62, v63\n"
"s_setprio 0\n"
"v_exp_f32 v32, v32\n"
"v_exp_f32 v33, v33\n"
"v_exp_f32 v34, v34\n"
"v_exp_f32 v35, v35\n"
"v_exp_f32 v36, v36\n"
"v_exp_f32 v37, v37\n"
"v_exp_f32 v38, v38\n"
"v_exp_f32 v39, v39\n"
"s_waitcnt vmcnt(3) lgkmcnt(0)\n"
"s_barrier\n"
"v_mfma_f32_32x32x16_bf16 v[48:63], v[80:83], v[8:11], v[16:31]\n"
"s_setprio 2\n"
"ds_read_b128 v[80:83], v232 offset:0\n"
"v_cvt_pk_bf16_f32 v96, v32, v33\n"
"v_cvt_pk_bf16_f32 v97, v34, v35\n"
"v_exp_f32 v40, v40\n"
"v_exp_f32 v41, v41\n"
"s_add_u32 m0, s81, 16384\n"
"s_nop 0\n"
"buffer_load_dwordx4 v233, s[12:15], s80 offen sc1 lds\n"
"v_mfma_f32_32x32x16_bf16 v[48:63], v[84:87], v[12:15], v[48:63]\n"
"ds_read_b128 v[84:87], v232 offset:1024\n"
"v_cvt_pk_bf16_f32 v98, v36, v37\n"
"v_cvt_pk_bf16_f32 v99, v38, v39\n"
"v_exp_f32 v42, v42\n"
"v_exp_f32 v43, v43\n"
"v_mfma_f32_32x32x16_bf16 v[64:79], v[92:95], v[100:103], v[64:79]\n"
"ds_read_b128 v[92:95], v232 offset:3072\n"
"v_exp_f32 v44, v44\n"
"v_exp_f32 v45, v45\n"
"v_cvt_pk_bf16_f32 v100, v40, v41\n"
"v_cvt_pk_bf16_f32 v101, v42, v43\n"
"buffer_load_dwordx4 v233, s[12:15], s80 offen offset:1024 sc1 lds\n"
"v_mfma_f32_32x32x16_bf16 v[64:79], v[88:91], v[96:99], v[64:79]\n"
"ds_read_b128 v[88:91], v232 offset:2048\n"
"v_exp_f32 v46, v46\n"
"v_exp_f32 v47, v47\n"
"v_cvt_pk_bf16_f32 v102, v44, v45\n"
"v_cvt_pk_bf16_f32 v103, v46, v47\n"
"s_setprio 0\n"
"v_exp_f32 v48, v48\n"
"v_exp_f32 v49, v49\n"
"v_exp_f32 v50, v50\n"
"v_exp_f32 v51, v51\n"
"v_exp_f32 v52, v52\n"
"v_exp_f32 v53, v53\n"
"v_exp_f32 v54, v54\n"
"v_exp_f32 v55, v55\n"
"s_waitcnt lgkmcnt(0)\n"
"v_mfma_f32_32x32x16_bf16 v[32:47], v[80:83], v[8:11], v[16:31]\n"
"s_setprio 2\n"
"ds_read_b128 v[80:83], v232 offset:4096\n"
"v_cvt_pk_bf16_f32 v96, v48, v49\n"
"v_cvt_pk_bf16_f32 v97, v50, v51\n"
"v_exp_f32 v56, v56\n"
"v_exp_f32 v57, v57\n"
"buffer_load_dwordx4 v234, s[12:15], s80 offen offset:2048 sc1 lds\n"
"v_mfma_f32_32x32x16_bf16 v[32:47], v[84:87], v[12:15], v[32:47]\n"
"ds_read_b128 v[84:87], v232 offset:5120\n"
"v_cvt_pk_bf16_f32 v98, v52, v53\n"
"v_cvt_pk_bf16_f32 v99, v54, v55\n"
"v_exp_f32 v58, v58\n"
"v_exp_f32 v59, v59\n"
"v_mfma_f32_32x32x16_bf16 v[64:79], v[92:95], v[100:103], v[64:79]\n"
"ds_read_b128 v[92:95], v232 offset:7168\n"
"v_exp_f32 v60, v60\n"
"v_exp_f32 v61, v61\n"
"v_cvt_pk_bf16_f32 v100, v56, v57\n"
"v_cvt_pk_bf16_f32 v101, v58, v59\n"
"buffer_load_dwordx4 v235, s[12:15], s80 offen offset:3072 sc1 lds\n"
"s_add_u32 s80, s80, 233472\n"
"v_mfma_f32_32x32x16_bf16 v[64:79], v[88:91], v[96:99], v[64:79]\n"
"ds_read_b128 v[88:91], v232 offset:6144\n"
"v_exp_f32 v62, v62\n"
"v_exp_f32 v63, v63\n"
"v_cvt_pk_bf16_f32 v102, v60, v61\n"
"v_cvt_pk_bf16_f32 v103, v62, v63\n"
"s_waitcnt vmcnt(4)\n"
"v_cmp_ne_u64 vcc, s[66:67], v[4:5]\n"
"s_setprio 0\n"
"v_exp_f32 v32, v32\n"
"v_exp_f32 v33, v33\n"
"v_exp_f32 v34, v34\n"
"v_exp_f32 v35, v35\n"
"v_exp_f32 v36, v36\n"
"v_exp_f32 v37, v37\n"
"v_exp_f32 v38, v38\n"
"v_exp_f32 v39, v39\n"
"s_add_u32 m0, s65, 140288\n"
"s_nop 0\n"
"buffer_load_dwordx4 v225, s[48:51], s73 offen lds\n"
"s_cbranch_vccz L_ft_33_%=\n"
"L_ftr_34_%=:\n"
"s_cmp_eq_u32 s85, 0\n"
"s_cbranch_scc1 L_ft_33_%=\n"
"s_sub_u32 s85, s85, 1\n"
"s_sleep 1\n"
"buffer_load_dwordx2 v[4:5], v250, s[12:15], 0 offen sc1\n"
"s_waitcnt vmcnt(0)\n"
"v_cmp_ne_u64 vcc, s[66:67], v[4:5]\n"
"s_nop 4\n"
"s_cbranch_vccnz L_ftr_34_%=\n"
"L_ft_33_%=:\n"
"s_add_u32 m0, s65, 123904\n"
"v_xor_b32 v7, 64, v242\n"
"buffer_load_dwordx4 v241, s[12:15], 0 offen sc1 lds\n"
"s_waitcnt lgkmcnt(0)\n"
"v_mfma_f32_32x32x16_bf16 v[48:63], v[80:83], v[8:11], v[16:31]\n"
"s_setprio 2\n"
"ds_read_b128 v[80:83], v232 offset:8192\n"
"v_cvt_pk_bf16_f32 v96, v32, v33\n"
"v_cvt_pk_bf16_f32 v97, v34, v35\n"
"v_exp_f32 v40, v40\n"
"v_exp_f32 v41, v41\n"
"s_mov_b32 m0, s88\n"
"s_nop 0\n"
"buffer_load_dwordx4 v253, s[24:27], s82 offen lds\n"
"s_add_u32 m0, s65, 94208\n"
"ds_read_b64_tr_b16 v[104:105], v228 offset:0\n"
"v_mfma_f32_32x32x16_bf16 v[48:63], v[84:87], v[12:15], v[48:63]\n"
"ds_read_b128 v[84:87], v232 offset:9216\n"
"v_cvt_pk_bf16_f32 v98, v36, v37\n"
"v_cvt_pk_bf16_f32 v99, v38, v39\n"
"v_exp_f32 v42, v42\n"
"v_exp_f32 v43, v43\n"
"ds_read_b64_tr_b16 v[106:107], v229 offset:0\n"
"v_mfma_f32_32x32x16_bf16 v[64:79], v[92:95], v[100:103], v[64:79]\n"
"ds_read_b128 v[92:95], v232 offset:11264\n"
"v_exp_f32 v44, v44\n"
"v_exp_f32 v45, v45\n"
"v_cvt_pk_bf16_f32 v100, v40, v41\n"
"v_cvt_pk_bf16_f32 v101, v42, v43\n"
"buffer_load_dwordx4 v240, s[24:27], s82 offen lds\n"
"s_add_u32 s82, s82, 50176\n"
"ds_read_b64_tr_b16 v[108:109], v230 offset:0\n"
"v_mfma_f32_32x32x16_bf16 v[64:79], v[88:91], v[96:99], v[64:79]\n"
"ds_read_b128 v[88:91], v232 offset:10240\n"
"v_exp_f32 v46, v46\n"
"v_exp_f32 v47, v47\n"
"v_cvt_pk_bf16_f32 v102, v44, v45\n"
"v_cvt_pk_bf16_f32 v103, v46, v47\n"
"ds_read_b64_tr_b16 v[110:111], v231 offset:0\n"
"s_setprio 0\n"
"v_exp_f32 v48, v48\n"
"v_exp_f32 v49, v49\n"
"v_exp_f32 v50, v50\n"
"v_exp_f32 v51, v51\n"
"v_exp_f32 v52, v52\n"
"v_exp_f32 v53, v53\n"
"v_exp_f32 v54, v54\n"
"v_exp_f32 v55, v55\n"
"s_waitcnt lgkmcnt(0)\n"
"v_mfma_f32_32x32x16_bf16 v[32:47], v[80:83], v[8:11], v[16:31]\n"
"s_setprio 2\n"
"ds_read_b128 v[80:83], v232 offset:12288\n"
"v_cvt_pk_bf16_f32 v96, v48, v49\n"
"v_cvt_pk_bf16_f32 v97, v50, v51\n"
"v_exp_f32 v56, v56\n"
"v_exp_f32 v57, v57\n"
"buffer_load_dwordx4 v[168:171], v0, s[20:23], s84 offen offset:0\n"
"v_mfma_f32_32x32x16_bf16 v[32:47], v[84:87], v[12:15], v[32:47]\n"
"ds_read_b128 v[84:87], v232 offset:13312\n"
"v_cvt_pk_bf16_f32 v98, v52, v53\n"
"v_cvt_pk_bf16_f32 v99, v54, v55\n"
"v_exp_f32 v58, v58\n"
"v_exp_f32 v59, v59\n"
"v_mfma_f32_32x32x16_bf16 v[64:79], v[92:95], v[100:103], v[64:79]\n"
"ds_read_b128 v[92:95], v232 offset:15360\n"
"v_exp_f32 v60, v60\n"
"v_exp_f32 v61, v61\n"
"v_cvt_pk_bf16_f32 v100, v56, v57\n"
"v_cvt_pk_bf16_f32 v101, v58, v59\n"
"buffer_load_dwordx4 v[172:175], v0, s[20:23], s84 offen offset:1024\n"
"v_mfma_f32_32x32x16_bf16 v[64:79], v[88:91], v[96:99], v[64:79]\n"
"ds_read_b128 v[88:91], v232 offset:14336\n"
"v_exp_f32 v62, v62\n"
"v_exp_f32 v63, v63\n"
"v_cvt_pk_bf16_f32 v102, v60, v61\n"
"v_cvt_pk_bf16_f32 v103, v62, v63\n"
"s_setprio 0\n"
"v_exp_f32 v32, v32\n"
"v_exp_f32 v33, v33\n"
"v_exp_f32 v34, v34\n"
"v_exp_f32 v35, v35\n"
"v_exp_f32 v36, v36\n"
"v_exp_f32 v37, v37\n"
"v_exp_f32 v38, v38\n"
"v_exp_f32 v39, v39\n"
"s_waitcnt vmcnt(6) lgkmcnt(0)\n"
"s_barrier\n"
"v_mfma_f32_32x32x16_bf16 v[48:63], v[80:83], v[8:11], v[16:31]\n"
"s_setprio 2\n"
"ds_read_b128 v[80:83], v232 offset:16384\n"
"v_cvt_pk_bf16_f32 v96, v32, v33\n"
"v_cvt_pk_bf16_f32 v97, v34, v35\n"
"v_exp_f32 v40, v40\n"
"v_exp_f32 v41, v41\n"
"s_add_u32 m0, s81, 0\n"
"s_nop 0\n"
"buffer_load_dwordx4 v233, s[12:15], s80 offen sc1 lds\n"
"ds_read_b64_tr_b16 v[112:113], v228 offset:14336\n"
"v_mfma_f32_32x32x16_bf16 v[48:63], v[84:87], v[12:15], v[48:63]\n"
"ds_read_b128 v[84:87], v232 offset:17408\n"
"v_cvt_pk_bf16_f32 v98, v36, v37\n"
"v_cvt_pk_bf16_f32 v99, v38, v39\n"
"v_exp_f32 v42, v42\n"
"v_exp_f32 v43, v43\n"
"ds_read_b64_tr_b16 v[114:115], v229 offset:14336\n"
"v_mfma_f32_32x32x16_bf16 v[64:79], v[92:95], v[100:103], v[64:79]\n"
"ds_read_b128 v[92:95], v232 offset:19456\n"
"v_exp_f32 v44, v44\n"
"v_exp_f32 v45, v45\n"
"v_cvt_pk_bf16_f32 v100, v40, v41\n"
"v_cvt_pk_bf16_f32 v101, v42, v43\n"
"buffer_load_dwordx4 v233, s[12:15], s80 offen offset:1024 sc1 lds\n"
"ds_read_b64_tr_b16 v[116:117], v230 offset:14336\n"
"v_mfma_f32_32x32x16_bf16 v[64:79], v[88:91], v[96:99], v[64:79]\n"
"ds_read_b128 v[88:91], v232 offset:18432\n"
"v_exp_f32 v46, v46\n"
"v_exp_f32 v47, v47\n"
"v_cvt_pk_bf16_f32 v102, v44, v45\n"
"v_cvt_pk_bf16_f32 v103, v46, v47\n"
"ds_read_b64_tr_b16 v[118:119], v231 offset:14336\n"
"s_setprio 0\n"
"v_exp_f32 v48, v48\n"
"v_exp_f32 v49, v49\n"
"v_exp_f32 v50, v50\n"
"v_exp_f32 v51, v51\n"
"v_exp_f32 v52, v52\n"
"v_exp_f32 v53, v53\n"
"v_exp_f32 v54, v54\n"
"v_exp_f32 v55, v55\n"
"s_waitcnt vmcnt(6) lgkmcnt(0)\n"
"s_barrier\n"
"v_mfma_f32_32x32x16_bf16 v[32:47], v[80:83], v[8:11], v[16:31]\n"
"s_setprio 2\n"
"ds_read_b128 v[80:83], v232 offset:20480\n"
"v_cvt_pk_bf16_f32 v96, v48, v49\n"
"v_cvt_pk_bf16_f32 v97, v50, v51\n"
"v_exp_f32 v56, v56\n"
"v_exp_f32 v57, v57\n"
"buffer_load_dwordx4 v234, s[12:15], s80 offen offset:2048 sc1 lds\n"
"ds_read_b64_tr_b16 v[200:201], v242 offset:0\n"
"ds_read_b64_tr_b16 v[202:203], v242 offset:512\n"
"v_mfma_f32_32x32x16_bf16 v[32:47], v[84:87], v[12:15], v[32:47]\n"
"ds_read_b128 v[84:87], v232 offset:21504\n"
"v_cvt_pk_bf16_f32 v98, v52, v53\n"
"v_cvt_pk_bf16_f32 v99, v54, v55\n"
"v_exp_f32 v58, v58\n"
"v_exp_f32 v59, v59\n"
"ds_read_b64_tr_b16 v[204:205], v242 offset:4096\n"
"ds_read_b64_tr_b16 v[206:207], v242 offset:4608\n"
"v_mfma_f32_32x32x16_bf16 v[64:79], v[92:95], v[100:103], v[64:79]\n"
"ds_read_b128 v[92:95], v232 offset:23552\n"
"v_exp_f32 v60, v60\n"
"v_exp_f32 v61, v61\n"
"v_cvt_pk_bf16_f32 v100, v56, v57\n"
"v_cvt_pk_bf16_f32 v101, v58, v59\n"
"buffer_load_dwordx4 v235, s[12:15], s80 offen offset:3072 sc1 lds\n"
"s_add_u32 s80, s80, 233472\n"
"ds_read_b64_tr_b16 v[208:209], v7 offset:0\n"
"ds_read_b64_tr_b16 v[210:211], v7 offset:512\n"
"v_mfma_f32_32x32x16_bf16 v[64:79], v[88:91], v[96:99], v[64:79]\n"
"ds_read_b128 v[88:91], v232 offset:22528\n"
"v_exp_f32 v62, v62\n"
"v_exp_f32 v63, v63\n"
"v_cvt_pk_bf16_f32 v102, v60, v61\n"
"v_cvt_pk_bf16_f32 v103, v62, v63\n"
"s_mov_b32 m0, s86\n"
"ds_read_b64_tr_b16 v[212:213], v7 offset:4096\n"
"ds_read_b64_tr_b16 v[214:215], v7 offset:4608\n"
"s_setprio 0\n"
"v_exp_f32 v32, v32\n"
"v_exp_f32 v33, v33\n"
"v_exp_f32 v34, v34\n"
"v_exp_f32 v35, v35\n"
"v_exp_f32 v36, v36\n"
"v_exp_f32 v37, v37\n"
"v_exp_f32 v38, v38\n"
"v_exp_f32 v39, v39\n"
"s_waitcnt lgkmcnt(0)\n"
"v_mfma_f32_32x32x16_bf16 v[48:63], v[80:83], v[8:11], v[16:31]\n"
"s_setprio 2\n"
"ds_read_b128 v[80:83], v232 offset:24576\n"
"v_cvt_pk_bf16_f32 v96, v32, v33\n"
"v_cvt_pk_bf16_f32 v97, v34, v35\n"
"v_exp_f32 v40, v40\n"
"v_exp_f32 v41, v41\n"
"buffer_load_dwordx4 v253, s[24:27], s82 offen lds\n"
"s_add_u32 m0, s65, 65536\n"
"ds_read_u16_d16_hi v160, v246 offset:0\n"
"ds_read_u16_d16_hi v161, v246 offset:64\n"
"v_mfma_f32_16x16x32_bf16 v[216:219], v[136:139], v[200:203], 0\n"
"v_mfma_f32_16x16x32_bf16 v[220:223], v[136:139], v[208:211], 0\n"
"v_mfma_f32_32x32x16_bf16 v[48:63], v[84:87], v[12:15], v[48:63]\n"
"ds_read_b128 v[84:87], v232 offset:25600\n"
"v_cvt_pk_bf16_f32 v98, v36, v37\n"
"v_cvt_pk_bf16_f32 v99, v38, v39\n"
"v_exp_f32 v42, v42\n"
"v_exp_f32 v43, v43\n"
"ds_read_u16_d16_hi v162, v246 offset:128\n"
"ds_read_u16_d16_hi v163, v246 offset:192\n"
"v_mfma_f32_16x16x32_bf16 v[216:219], v[144:147], v[208:211], v[216:219]\n"
"v_mfma_f32_16x16x32_bf16 v[220:223], v[152:155], v[200:203], v[220:223]\n"
"v_mfma_f32_32x32x16_bf16 v[64:79], v[92:95], v[100:103], v[64:79]\n"
"ds_read_b128 v[92:95], v232 offset:27648\n"
"v_exp_f32 v44, v44\n"
"v_exp_f32 v45, v45\n"
"v_cvt_pk_bf16_f32 v100, v40, v41\n"
"v_cvt_pk_bf16_f32 v101, v42, v43\n"
"buffer_load_dwordx4 v240, s[24:27], s82 offen lds\n"
"s_add_u32 s82, s82, 50176\n"
"ds_read_u16_d16_hi v164, v246 offset:4096\n"
"ds_read_u16_d16_hi v165, v246 offset:4160\n"
"v_mfma_f32_16x16x32_bf16 v[216:219], v[140:143], v[204:207], v[216:219]\n"
"v_mfma_f32_16x16x32_bf16 v[220:223], v[140:143], v[212:215], v[220:223]\n"
"v_mfma_f32_32x32x16_bf16 v[64:79], v[88:91], v[96:99], v[64:79]\n"
"ds_read_b128 v[88:91], v232 offset:26624\n"
"v_exp_f32 v46, v46\n"
"v_exp_f32 v47, v47\n"
"v_cvt_pk_bf16_f32 v102, v44, v45\n"
"v_cvt_pk_bf16_f32 v103, v46, v47\n"
"ds_read_u16_d16_hi v166, v246 offset:4224\n"
"ds_read_u16_d16_hi v167, v246 offset:4288\n"
"v_mfma_f32_16x16x32_bf16 v[216:219], v[148:151], v[212:215], v[216:219]\n"
"v_mfma_f32_16x16x32_bf16 v[220:223], v[156:159], v[204:207], v[220:223]\n"
"s_setprio 0\n"
"v_exp_f32 v48, v48\n"
"v_exp_f32 v49, v49\n"
"v_exp_f32 v50, v50\n"
"v_exp_f32 v51, v51\n"
"v_exp_f32 v52, v52\n"
"v_exp_f32 v53, v53\n"
"v_exp_f32 v54, v54\n"
"v_exp_f32 v55, v55\n"
"s_waitcnt lgkmcnt(0)\n"
"v_mfma_f32_32x32x16_bf16 v[32:47], v[80:83], v[8:11], v[16:31]\n"
"s_setprio 2\n"
"ds_read_b128 v[80:83], v232 offset:28672\n"
"v_cvt_pk_bf16_f32 v96, v48, v49\n"
"v_cvt_pk_bf16_f32 v97, v50, v51\n"
"v_exp_f32 v56, v56\n"
"v_exp_f32 v57, v57\n"
"v_pk_mul_f32 v[200:201], v[216:217], v[160:161]\n"
"v_pk_mul_f32 v[202:203], v[218:219], v[162:163]\n"
"v_pk_mul_f32 v[204:205], v[216:217], v[164:165]\n"
"v_pk_mul_f32 v[206:207], v[218:219], v[166:167]\n"
"buffer_load_dwordx4 v[176:179], v0, s[20:23], s84 offen offset:2048\n"
"v_mfma_f32_32x32x16_bf16 v[32:47], v[84:87], v[12:15], v[32:47]\n"
"ds_read_b128 v[84:87], v232 offset:29696\n"
"v_cvt_pk_bf16_f32 v98, v52, v53\n"
"v_cvt_pk_bf16_f32 v99, v54, v55\n"
"v_exp_f32 v58, v58\n"
"v_exp_f32 v59, v59\n"
"v_pk_fma_f32 v[200:201], v[220:221], v[164:165], v[200:201] neg_lo:[1,0,0] neg_hi:[1,0,0]\n"
"v_pk_fma_f32 v[202:203], v[222:223], v[166:167], v[202:203] neg_lo:[1,0,0] neg_hi:[1,0,0]\n"
"v_pk_fma_f32 v[204:205], v[220:221], v[160:161], v[204:205]\n"
"v_pk_fma_f32 v[206:207], v[222:223], v[162:163], v[206:207]\n"
"v_mfma_f32_32x32x16_bf16 v[64:79], v[92:95], v[100:103], v[64:79]\n"
"ds_read_b128 v[92:95], v232 offset:31744\n"
"v_exp_f32 v60, v60\n"
"v_exp_f32 v61, v61\n"
"v_cvt_pk_bf16_f32 v100, v56, v57\n"
"v_cvt_pk_bf16_f32 v101, v58, v59\n"
"v_cvt_pk_bf16_f32 v200, v200, v201\n"
"v_cvt_pk_bf16_f32 v201, v202, v203\n"
"v_cvt_pk_bf16_f32 v204, v204, v205\n"
"v_cvt_pk_bf16_f32 v205, v206, v207\n"
"buffer_load_dwordx4 v[180:183], v0, s[20:23], s84 offen offset:3072\n"
"v_mfma_f32_32x32x16_bf16 v[64:79], v[88:91], v[96:99], v[64:79]\n"
"ds_read_b128 v[88:91], v232 offset:30720\n"
"v_exp_f32 v62, v62\n"
"v_exp_f32 v63, v63\n"
"v_cvt_pk_bf16_f32 v102, v60, v61\n"
"v_cvt_pk_bf16_f32 v103, v62, v63\n"
"ds_write_b64 v243, v[200:201]\n"
"ds_write_b64 v243, v[204:205] offset:2048\n"
"s_setprio 0\n"
"v_exp_f32 v32, v32\n"
"v_exp_f32 v33, v33\n"
"v_exp_f32 v34, v34\n"
"v_exp_f32 v35, v35\n"
"v_exp_f32 v36, v36\n"
"v_exp_f32 v37, v37\n"
"v_exp_f32 v38, v38\n"
"v_exp_f32 v39, v39\n"
"s_waitcnt vmcnt(4) lgkmcnt(0)\n"
"s_barrier\n"
"v_mfma_f32_32x32x16_bf16 v[48:63], v[80:83], v[8:11], v[16:31]\n"
"s_setprio 2\n"
"ds_read_b128 v[80:83], v232 offset:0\n"
"v_cvt_pk_bf16_f32 v96, v32, v33\n"
"v_cvt_pk_bf16_f32 v97, v34, v35\n"
"v_exp_f32 v40, v40\n"
"v_exp_f32 v41, v41\n"
"s_mov_b32 m0, s87\n"
"s_nop 0\n"
"buffer_load_dwordx4 v253, s[24:27], s82 offen lds\n"
"s_add_u32 m0, s65, 79872\n"
"ds_read_b128 v[200:203], v244 offset:0\n"
"v_mfma_f32_32x32x16_bf16 v[48:63], v[84:87], v[12:15], v[48:63]\n"
"ds_read_b128 v[84:87], v232 offset:1024\n"
"v_cvt_pk_bf16_f32 v98, v36, v37\n"
"v_cvt_pk_bf16_f32 v99, v38, v39\n"
"v_exp_f32 v42, v42\n"
"v_exp_f32 v43, v43\n"
"buffer_load_dwordx4 v240, s[24:27], s82 offen lds\n"
"s_add_u32 s82, s82, 50176\n"
"ds_read_b128 v[204:207], v245 offset:0\n"
"v_mfma_f32_32x32x16_bf16 v[64:79], v[92:95], v[100:103], v[64:79]\n"
"ds_read_b128 v[92:95], v232 offset:3072\n"
"v_exp_f32 v44, v44\n"
"v_exp_f32 v45, v45\n"
"v_cvt_pk_bf16_f32 v100, v40, v41\n"
"v_cvt_pk_bf16_f32 v101, v42, v43\n"
"ds_read_b128 v[208:211], v244 offset:2048\n"
"v_mfma_f32_32x32x16_bf16 v[64:79], v[88:91], v[96:99], v[64:79]\n"
"ds_read_b128 v[88:91], v232 offset:2048\n"
"v_exp_f32 v46, v46\n"
"v_exp_f32 v47, v47\n"
"v_cvt_pk_bf16_f32 v102, v44, v45\n"
"v_cvt_pk_bf16_f32 v103, v46, v47\n"
"s_add_u32 m0, s81, 16384\n"
"s_nop 0\n"
"buffer_load_dwordx4 v233, s[12:15], s80 offen sc1 lds\n"
"ds_read_b128 v[212:215], v245 offset:2048\n"
"s_setprio 0\n"
"v_exp_f32 v48, v48\n"
"v_exp_f32 v49, v49\n"
"v_exp_f32 v50, v50\n"
"v_exp_f32 v51, v51\n"
"v_exp_f32 v52, v52\n"
"v_exp_f32 v53, v53\n"
"v_exp_f32 v54, v54\n"
"v_exp_f32 v55, v55\n"
"s_waitcnt lgkmcnt(0)\n"
"v_mfma_f32_32x32x16_bf16 v[32:47], v[80:83], v[8:11], v[16:31]\n"
"s_setprio 2\n"
"ds_read_b128 v[80:83], v232 offset:4096\n"
"v_cvt_pk_bf16_f32 v96, v48, v49\n"
"v_cvt_pk_bf16_f32 v97, v50, v51\n"
"v_exp_f32 v56, v56\n"
"v_exp_f32 v57, v57\n"
"buffer_load_dwordx4 v233, s[12:15], s80 offen offset:1024 sc1 lds\n"
"v_mfma_f32_16x16x32_bf16 v[216:219], v[200:203], v[136:139], 0\n"
"v_mfma_f32_16x16x32_bf16 v[220:223], v[208:211], v[136:139], 0\n"
"v_mfma_f32_32x32x16_bf16 v[32:47], v[84:87], v[12:15], v[32:47]\n"
"ds_read_b128 v[84:87], v232 offset:5120\n"
"v_cvt_pk_bf16_f32 v98, v52, v53\n"
"v_cvt_pk_bf16_f32 v99, v54, v55\n"
"v_exp_f32 v58, v58\n"
"v_exp_f32 v59, v59\n"
"buffer_load_dwordx4 v234, s[12:15], s80 offen offset:2048 sc1 lds\n"
"v_mfma_f32_16x16x32_bf16 v[216:219], v[208:211], v[152:155], v[216:219]\n"
"v_mfma_f32_16x16x32_bf16 v[220:223], v[200:203], v[144:147], v[220:223]\n"
"v_mfma_f32_32x32x16_bf16 v[64:79], v[92:95], v[100:103], v[64:79]\n"
"ds_read_b128 v[92:95], v232 offset:7168\n"
"v_exp_f32 v60, v60\n"
"v_exp_f32 v61, v61\n"
"v_cvt_pk_bf16_f32 v100, v56, v57\n"
"v_cvt_pk_bf16_f32 v101, v58, v59\n"
"v_mfma_f32_16x16x32_bf16 v[216:219], v[204:207], v[140:143], v[216:219]\n"
"v_mfma_f32_16x16x32_bf16 v[220:223], v[212:215], v[140:143], v[220:223]\n"
"v_mfma_f32_32x32x16_bf16 v[64:79], v[88:91], v[96:99], v[64:79]\n"
"ds_read_b128 v[88:91], v232 offset:6144\n"
"v_exp_f32 v62, v62\n"
"v_exp_f32 v63, v63\n"
"v_cvt_pk_bf16_f32 v102, v60, v61\n"
"v_cvt_pk_bf16_f32 v103, v62, v63\n"
"buffer_load_dwordx4 v235, s[12:15], s80 offen offset:3072 sc1 lds\n"
"s_add_u32 s80, s80, 233472\n"
"v_mfma_f32_16x16x32_bf16 v[216:219], v[212:215], v[156:159], v[216:219]\n"
"v_mfma_f32_16x16x32_bf16 v[220:223], v[204:207], v[148:151], v[220:223]\n"
"s_setprio 0\n"
"v_exp_f32 v32, v32\n"
"v_exp_f32 v33, v33\n"
"v_exp_f32 v34, v34\n"
"v_exp_f32 v35, v35\n"
"v_exp_f32 v36, v36\n"
"v_exp_f32 v37, v37\n"
"v_exp_f32 v38, v38\n"
"v_exp_f32 v39, v39\n"
"s_waitcnt lgkmcnt(0)\n"
"v_mfma_f32_32x32x16_bf16 v[48:63], v[80:83], v[8:11], v[16:31]\n"
"s_setprio 2\n"
"ds_read_b128 v[80:83], v232 offset:8192\n"
"v_cvt_pk_bf16_f32 v96, v32, v33\n"
"v_cvt_pk_bf16_f32 v97, v34, v35\n"
"v_exp_f32 v40, v40\n"
"v_exp_f32 v41, v41\n"
"s_bitcmp1_b32 s63, 0\n"
"s_cbranch_scc1 L_odd_35_%=\n"
"v_cvt_pk_bf16_f32 v200, v216, v217\n"
"v_cvt_pk_bf16_f32 v201, v218, v219\n"
"v_cvt_pk_bf16_f32 v202, v220, v221\n"
"v_cvt_pk_bf16_f32 v203, v222, v223\n"
"s_branch L_evn_36_%=\n"
"L_odd_35_%=:\n"
"v_cvt_pk_bf16_f32 v200, v220, v221\n"
"v_cvt_pk_bf16_f32 v201, v222, v223\n"
"v_cvt_pk_bf16_f32 v202, v216, v217\n"
"v_cvt_pk_bf16_f32 v203, v218, v219\n"
"L_evn_36_%=:\n"
"buffer_store_dwordx4 v[200:203], v247, s[12:15], 0 offen sc1\n"
"v_mfma_f32_32x32x16_bf16 v[48:63], v[84:87], v[12:15], v[48:63]\n"
"ds_read_b128 v[84:87], v232 offset:9216\n"
"v_cvt_pk_bf16_f32 v98, v36, v37\n"
"v_cvt_pk_bf16_f32 v99, v38, v39\n"
"v_exp_f32 v42, v42\n"
"v_exp_f32 v43, v43\n"
"v_mfma_f32_32x32x16_bf16 v[64:79], v[92:95], v[100:103], v[64:79]\n"
"ds_read_b128 v[92:95], v232 offset:11264\n"
"v_exp_f32 v44, v44\n"
"v_exp_f32 v45, v45\n"
"v_cvt_pk_bf16_f32 v100, v40, v41\n"
"v_cvt_pk_bf16_f32 v101, v42, v43\n"
"v_mfma_f32_32x32x16_bf16 v[64:79], v[88:91], v[96:99], v[64:79]\n"
"ds_read_b128 v[88:91], v232 offset:10240\n"
"v_exp_f32 v46, v46\n"
"v_exp_f32 v47, v47\n"
"v_cvt_pk_bf16_f32 v102, v44, v45\n"
"v_cvt_pk_bf16_f32 v103, v46, v47\n"
"s_setprio 0\n"
"v_exp_f32 v48, v48\n"
"v_exp_f32 v49, v49\n"
"v_exp_f32 v50, v50\n"
"v_exp_f32 v51, v51\n"
"v_exp_f32 v52, v52\n"
"v_exp_f32 v53, v53\n"
"v_exp_f32 v54, v54\n"
"v_exp_f32 v55, v55\n"
"s_waitcnt lgkmcnt(0)\n"
"v_mfma_f32_32x32x16_bf16 v[32:47], v[80:83], v[8:11], v[16:31]\n"
"s_setprio 2\n"
"ds_read_b128 v[80:83], v232 offset:12288\n"
"v_cvt_pk_bf16_f32 v96, v48, v49\n"
"v_cvt_pk_bf16_f32 v97, v50, v51\n"
"v_exp_f32 v56, v56\n"
"v_exp_f32 v57, v57\n"
"ds_read_b64_tr_b16 v[120:121], v228 offset:28672\n"
"v_mfma_f32_32x32x16_bf16 v[32:47], v[84:87], v[12:15], v[32:47]\n"
"ds_read_b128 v[84:87], v232 offset:13312\n"
"v_cvt_pk_bf16_f32 v98, v52, v53\n"
"v_cvt_pk_bf16_f32 v99, v54, v55\n"
"v_exp_f32 v58, v58\n"
"v_exp_f32 v59, v59\n"
"ds_read_b64_tr_b16 v[122:123], v229 offset:28672\n"
"v_mfma_f32_32x32x16_bf16 v[64:79], v[92:95], v[100:103], v[64:79]\n"
"ds_read_b128 v[92:95], v232 offset:15360\n"
"v_exp_f32 v60, v60\n"
"v_exp_f32 v61, v61\n"
"v_cvt_pk_bf16_f32 v100, v56, v57\n"
"v_cvt_pk_bf16_f32 v101, v58, v59\n"
"ds_read_b64_tr_b16 v[124:125], v230 offset:28672\n"
"v_mfma_f32_32x32x16_bf16 v[64:79], v[88:91], v[96:99], v[64:79]\n"
"ds_read_b128 v[88:91], v232 offset:14336\n"
"v_exp_f32 v62, v62\n"
"v_exp_f32 v63, v63\n"
"v_cvt_pk_bf16_f32 v102, v60, v61\n"
"v_cvt_pk_bf16_f32 v103, v62, v63\n"
"ds_read_b64_tr_b16 v[126:127], v231 offset:28672\n"
"s_setprio 0\n"
"v_exp_f32 v32, v32\n"
"v_exp_f32 v33, v33\n"
"v_exp_f32 v34, v34\n"
"v_exp_f32 v35, v35\n"
"v_exp_f32 v36, v36\n"
"v_exp_f32 v37, v37\n"
"v_exp_f32 v38, v38\n"
"v_exp_f32 v39, v39\n"
"s_waitcnt vmcnt(1) lgkmcnt(0)\n"
"s_barrier\n"
"v_mfma_f32_32x32x16_bf16 v[48:63], v[80:83], v[8:11], v[16:31]\n"
"s_setprio 2\n"
"ds_read_b128 v[80:83], v232 offset:16384\n"
"v_cvt_pk_bf16_f32 v96, v32, v33\n"
"v_cvt_pk_bf16_f32 v97, v34, v35\n"
"v_exp_f32 v40, v40\n"
"v_exp_f32 v41, v41\n"
"s_add_u32 m0, s81, 0\n"
"s_nop 0\n"
"buffer_load_dwordx4 v233, s[12:15], s80 offen sc1 lds\n"
"v_mfma_f32_32x32x16_bf16 v[48:63], v[84:87], v[12:15], v[48:63]\n"
"ds_read_b128 v[84:87], v232 offset:17408\n"
"v_cvt_pk_bf16_f32 v98, v36, v37\n"
"v_cvt_pk_bf16_f32 v99, v38, v39\n"
"v_exp_f32 v42, v42\n"
"v_exp_f32 v43, v43\n"
"v_mfma_f32_32x32x16_bf16 v[64:79], v[92:95], v[100:103], v[64:79]\n"
"ds_read_b128 v[92:95], v232 offset:19456\n"
"v_exp_f32 v44, v44\n"
"v_exp_f32 v45, v45\n"
"v_cvt_pk_bf16_f32 v100, v40, v41\n"
"v_cvt_pk_bf16_f32 v101, v42, v43\n"
"buffer_load_dwordx4 v233, s[12:15], s80 offen offset:1024 sc1 lds\n"
"v_mfma_f32_32x32x16_bf16 v[64:79], v[88:91], v[96:99], v[64:79]\n"
"ds_read_b128 v[88:91], v232 offset:18432\n"
"v_exp_f32 v46, v46\n"
"v_exp_f32 v47, v47\n"
"v_cvt_pk_bf16_f32 v102, v44, v45\n"
"v_cvt_pk_bf16_f32 v103, v46, v47\n"
"s_setprio 0\n"
"v_exp_f32 v48, v48\n"
"v_exp_f32 v49, v49\n"
"v_exp_f32 v50, v50\n"
"v_exp_f32 v51, v51\n"
"v_exp_f32 v52, v52\n"
"v_exp_f32 v53, v53\n"
"v_exp_f32 v54, v54\n"
"v_exp_f32 v55, v55\n"
"s_waitcnt lgkmcnt(0)\n"
"v_mfma_f32_32x32x16_bf16 v[32:47], v[80:83], v[8:11], v[16:31]\n"
"s_setprio 2\n"
"ds_read_b128 v[80:83], v232 offset:20480\n"
"v_cvt_pk_bf16_f32 v96, v48, v49\n"
"v_cvt_pk_bf16_f32 v97, v50, v51\n"
"v_exp_f32 v56, v56\n"
"v_exp_f32 v57, v57\n"
"buffer_load_dwordx4 v234, s[12:15], s80 offen offset:2048 sc1 lds\n"
"v_mfma_f32_32x32x16_bf16 v[32:47], v[84:87], v[12:15], v[32:47]\n"
"ds_read_b128 v[84:87], v232 offset:21504\n"
"v_cvt_pk_bf16_f32 v98, v52, v53\n"
"v_cvt_pk_bf16_f32 v99, v54, v55\n"
"v_exp_f32 v58, v58\n"
"v_exp_f32 v59, v59\n"
"v_mfma_f32_32x32x16_bf16 v[64:79], v[92:95], v[100:103], v[64:79]\n"
"ds_read_b128 v[92:95], v232 offset:23552\n"
"v_exp_f32 v60, v60\n"
"v_exp_f32 v61, v61\n"
"v_cvt_pk_bf16_f32 v100, v56, v57\n"
"v_cvt_pk_bf16_f32 v101, v58, v59\n"
"buffer_load_dwordx4 v235, s[12:15], s80 offen offset:3072 sc1 lds\n"
"s_add_u32 s80, s80, 233472\n"
"v_mfma_f32_32x32x16_bf16 v[64:79], v[88:91], v[96:99], v[64:79]\n"
"ds_read_b128 v[88:91], v232 offset:22528\n"
"v_exp_f32 v62, v62\n"
"v_exp_f32 v63, v63\n"
"v_cvt_pk_bf16_f32 v102, v60, v61\n"
"v_cvt_pk_bf16_f32 v103, v62, v63\n"
"s_setprio 0\n"
"v_exp_f32 v32, v32\n"
"v_exp_f32 v33, v33\n"
"v_exp_f32 v34, v34\n"
"v_exp_f32 v35, v35\n"
"v_exp_f32 v36, v36\n"
"v_exp_f32 v37, v37\n"
"v_exp_f32 v38, v38\n"
"v_exp_f32 v39, v39\n"
"s_waitcnt vmcnt(4) lgkmcnt(0)\n"
"s_barrier\n"
"v_mfma_f32_32x32x16_bf16 v[48:63], v[80:83], v[8:11], v[16:31]\n"
"s_setprio 2\n"
"ds_read_b128 v[80:83], v232 offset:24576\n"
"v_cvt_pk_bf16_f32 v96, v32, v33\n"
"v_cvt_pk_bf16_f32 v97, v34, v35\n"
"v_exp_f32 v40, v40\n"
"v_exp_f32 v41, v41\n"
"ds_read_b64_tr_b16 v[128:129], v228 offset:0\n"
"s_cmp_lg_u32 s45, 7\n"
"s_cbranch_scc1 L_pf_37_%=\n"
"s_lshl_b32 s70, s63, 3\n"
"s_add_u32 s70, s70, s64\n"
"s_lshl_b32 s70, s70, 7\n"
"s_add_u32 s70, s70, 98304\n"
"s_mov_b64 exec, 1\n"
"buffer_store_dwordx2 v[254:255], off, s[12:15], s70 sc1\n"
"s_mov_b64 exec, -1\n"
"L_pf_37_%=:\n"
"v_mfma_f32_32x32x16_bf16 v[48:63], v[84:87], v[12:15], v[48:63]\n"
"ds_read_b128 v[84:87], v232 offset:25600\n"
"v_cvt_pk_bf16_f32 v98, v36, v37\n"
"v_cvt_pk_bf16_f32 v99, v38, v39\n"
"v_exp_f32 v42, v42\n"
"v_exp_f32 v43, v43\n"
"s_mov_b32 m0, s88\n"
"s_nop 0\n"
"buffer_load_dwordx4 v253, s[24:27], s82 offen lds\n"
"s_add_u32 m0, s65, 94208\n"
"ds_read_b64_tr_b16 v[130:131], v229 offset:0\n"
"v_mfma_f32_32x32x16_bf16 v[64:79], v[92:95], v[100:103], v[64:79]\n"
"ds_read_b128 v[92:95], v232 offset:27648\n"
"v_exp_f32 v44, v44\n"
"v_exp_f32 v45, v45\n"
"v_cvt_pk_bf16_f32 v100, v40, v41\n"
"v_cvt_pk_bf16_f32 v101, v42, v43\n"
"ds_read_b64_tr_b16 v[132:133], v230 offset:0\n"
"v_mfma_f32_32x32x16_bf16 v[64:79], v[88:91], v[96:99], v[64:79]\n"
"ds_read_b128 v[88:91], v232 offset:26624\n"
"v_exp_f32 v46, v46\n"
"v_exp_f32 v47, v47\n"
"v_cvt_pk_bf16_f32 v102, v44, v45\n"
"v_cvt_pk_bf16_f32 v103, v46, v47\n"
"buffer_load_dwordx4 v240, s[24:27], s82 offen lds\n"
"s_add_u32 s82, s82, 50176\n"
"ds_read_b64_tr_b16 v[134:135], v231 offset:0\n"
"s_setprio 0\n"
"v_exp_f32 v48, v48\n"
"v_exp_f32 v49, v49\n"
"v_exp_f32 v50, v50\n"
"v_exp_f32 v51, v51\n"
"v_exp_f32 v52, v52\n"
"v_exp_f32 v53, v53\n"
"v_exp_f32 v54, v54\n"
"v_exp_f32 v55, v55\n"
"s_waitcnt lgkmcnt(0)\n"
"v_mfma_f32_32x32x16_bf16 v[32:47], v[80:83], v[8:11], v[16:31]\n"
"s_setprio 2\n"
"ds_read_b128 v[80:83], v232 offset:28672\n"
"v_cvt_pk_bf16_f32 v96, v48, v49\n"
"v_cvt_pk_bf16_f32 v97, v50, v51\n"
"v_exp_f32 v56, v56\n"
"v_exp_f32 v57, v57\n"
"s_add_u32 s84, s84, 4096\n"
"buffer_load_dwordx4 v[184:187], v0, s[20:23], s84 offen offset:0\n"
"v_mfma_f32_32x32x16_bf16 v[32:47], v[84:87], v[12:15], v[32:47]\n"
"ds_read_b128 v[84:87], v232 offset:29696\n"
"v_cvt_pk_bf16_f32 v98, v52, v53\n"
"v_cvt_pk_bf16_f32 v99, v54, v55\n"
"v_exp_f32 v58, v58\n"
"v_exp_f32 v59, v59\n"
"v_mfma_f32_32x32x16_bf16 v[64:79], v[92:95], v[100:103], v[64:79]\n"
"ds_read_b128 v[92:95], v232 offset:31744\n"
"v_exp_f32 v60, v60\n"
"v_exp_f32 v61, v61\n"
"v_cvt_pk_bf16_f32 v100, v56, v57\n"
"v_cvt_pk_bf16_f32 v101, v58, v59\n"
"v_mfma_f32_32x32x16_bf16 v[64:79], v[88:91], v[96:99], v[64:79]\n"
"ds_read_b128 v[88:91], v232 offset:30720\n"
"v_exp_f32 v62, v62\n"
"v_exp_f32 v63, v63\n"
"v_cvt_pk_bf16_f32 v102, v60, v61\n"
"v_cvt_pk_bf16_f32 v103, v62, v63\n"
"s_setprio 0\n"
"v_exp_f32 v32, v32\n"
"v_exp_f32 v33, v33\n"
"v_exp_f32 v34, v34\n"
"v_exp_f32 v35, v35\n"
"v_exp_f32 v36, v36\n"
"v_exp_f32 v37, v37\n"
"v_exp_f32 v38, v38\n"
"v_exp_f32 v39, v39\n"
"s_waitcnt vmcnt(3) lgkmcnt(0)\n"
"s_barrier\n"
"v_mfma_f32_32x32x16_bf16 v[48:63], v[80:83], v[8:11], v[16:31]\n"
"s_setprio 2\n"
"ds_read_b128 v[80:83], v232 offset:0\n"
"v_cvt_pk_bf16_f32 v96, v32, v33\n"
"v_cvt_pk_bf16_f32 v97, v34, v35\n"
"v_exp_f32 v40, v40\n"
"v_exp_f32 v41, v41\n"
"s_add_u32 m0, s81, 16384\n"
"s_nop 0\n"
"buffer_load_dwordx4 v233, s[12:15], s80 offen sc1 lds\n"
"v_mfma_f32_32x32x16_bf16 v[48:63], v[84:87], v[12:15], v[48:63]\n"
"ds_read_b128 v[84:87], v232 offset:1024\n"
"v_cvt_pk_bf16_f32 v98, v36, v37\n"
"v_cvt_pk_bf16_f32 v99, v38, v39\n"
"v_exp_f32 v42, v42\n"
"v_exp_f32 v43, v43\n"
"v_mfma_f32_32x32x16_bf16 v[64:79], v[92:95], v[100:103], v[64:79]\n"
"ds_read_b128 v[92:95], v232 offset:3072\n"
"v_exp_f32 v44, v44\n"
"v_exp_f32 v45, v45\n"
"v_cvt_pk_bf16_f32 v100, v40, v41\n"
"v_cvt_pk_bf16_f32 v101, v42, v43\n"
"buffer_load_dwordx4 v233, s[12:15], s80 offen offset:1024 sc1 lds\n"
"v_mfma_f32_32x32x16_bf16 v[64:79], v[88:91], v[96:99], v[64:79]\n"
"ds_read_b128 v[88:91], v232 offset:2048\n"
"v_exp_f32 v46, v46\n"
"v_exp_f32 v47, v47\n"
"v_cvt_pk_bf16_f32 v102, v44, v45\n"
"v_cvt_pk_bf16_f32 v103, v46, v47\n"
"s_setprio 0\n"
"v_exp_f32 v48, v48\n"
"v_exp_f32 v49, v49\n"
"v_exp_f32 v50, v50\n"
"v_exp_f32 v51, v51\n"
"v_exp_f32 v52, v52\n"
"v_exp_f32 v53, v53\n"
"v_exp_f32 v54, v54\n"
"v_exp_f32 v55, v55\n"
"s_waitcnt lgkmcnt(0)\n"
"v_mfma_f32_32x32x16_bf16 v[32:47], v[80:83], v[8:11], v[16:31]\n"
"s_setprio 2\n"
"ds_read_b128 v[80:83], v232 offset:4096\n"
"v_cvt_pk_bf16_f32 v96, v48, v49\n"
"v_cvt_pk_bf16_f32 v97, v50, v51\n"
"v_exp_f32 v56, v56\n"
"v_exp_f32 v57, v57\n"
"buffer_load_dwordx4 v234, s[12:15], s80 offen offset:2048 sc1 lds\n"
"v_mfma_f32_32x32x16_bf16 v[32:47], v[84:87], v[12:15], v[32:47]\n"
"ds_read_b128 v[84:87], v232 offset:5120\n"
"v_cvt_pk_bf16_f32 v98, v52, v53\n"
"v_cvt_pk_bf16_f32 v99, v54, v55\n"
"v_exp_f32 v58, v58\n"
"v_exp_f32 v59, v59\n"
"v_mfma_f32_32x32x16_bf16 v[64:79], v[92:95], v[100:103], v[64:79]\n"
"ds_read_b128 v[92:95], v232 offset:7168\n"
"v_exp_f32 v60, v60\n"
"v_exp_f32 v61, v61\n"
"v_cvt_pk_bf16_f32 v100, v56, v57\n"
"v_cvt_pk_bf16_f32 v101, v58, v59\n"
"buffer_load_dwordx4 v235, s[12:15], s80 offen offset:3072 sc1 lds\n"
"s_add_u32 s80, s80, 233472\n"
"v_mfma_f32_32x32x16_bf16 v[64:79], v[88:91], v[96:99], v[64:79]\n"
"ds_read_b128 v[88:91], v232 offset:6144\n"
"v_exp_f32 v62, v62\n"
"v_exp_f32 v63, v63\n"
"v_cvt_pk_bf16_f32 v102, v60, v61\n"
"v_cvt_pk_bf16_f32 v103, v62, v63\n"
"s_mov_b32 m0, s86\n"
"s_setprio 0\n"
"v_exp_f32 v32, v32\n"
"v_exp_f32 v33, v33\n"
"v_exp_f32 v34, v34\n"
"v_exp_f32 v35, v35\n"
"v_exp_f32 v36, v36\n"
"v_exp_f32 v37, v37\n"
"v_exp_f32 v38, v38\n"
"v_exp_f32 v39, v39\n"
"s_waitcnt lgkmcnt(0)\n"
"v_mfma_f32_32x32x16_bf16 v[48:63], v[80:83], v[8:11], v[16:31]\n"
"s_setprio 2\n"
"ds_read_b128 v[80:83], v232 offset:8192\n"
"v_cvt_pk_bf16_f32 v96, v32, v33\n"
"v_cvt_pk_bf16_f32 v97, v34, v35\n"
"v_exp_f32 v40, v40\n"
"v_exp_f32 v41, v41\n"
"buffer_load_dwordx4 v253, s[24:27], s82 offen lds\n"
"s_add_u32 m0, s65, 65536\n"
"ds_read_b64_tr_b16 v[136:137], v228 offset:14336\n"
"v_mfma_f32_32x32x16_bf16 v[48:63], v[84:87], v[12:15], v[48:63]\n"
"ds_read_b128 v[84:87], v232 offset:9216\n"
"v_cvt_pk_bf16_f32 v98, v36, v37\n"
"v_cvt_pk_bf16_f32 v99, v38, v39\n"
"v_exp_f32 v42, v42\n"
"v_exp_f32 v43, v43\n"
"ds_read_b64_tr_b16 v[138:139], v229 offset:14336\n"
"v_mfma_f32_32x32x16_bf16 v[64:79], v[92:95], v[100:103], v[64:79]\n"
"ds_read_b128 v[92:95], v232 offset:11264\n"
"v_exp_f32 v44, v44\n"
"v_exp_f32 v45, v45\n"
"v_cvt_pk_bf16_f32 v100, v40, v41\n"
"v_cvt_pk_bf16_f32 v101, v42, v43\n"
"buffer_load_dwordx4 v240, s[24:27], s82 offen lds\n"
"s_add_u32 s82, s82, 50176\n"
"ds_read_b64_tr_b16 v[140:141], v230 offset:14336\n"
"v_mfma_f32_32x32x16_bf16 v[64:79], v[88:91], v[96:99], v[64:79]\n"
"ds_read_b128 v[88:91], v232 offset:10240\n"
"v_exp_f32 v46, v46\n"
"v_exp_f32 v47, v47\n"
"v_cvt_pk_bf16_f32 v102, v44, v45\n"
"v_cvt_pk_bf16_f32 v103, v46, v47\n"
"ds_read_b64_tr_b16 v[142:143], v231 offset:14336\n"
"s_setprio 0\n"
"v_exp_f32 v48, v48\n"
"v_exp_f32 v49, v49\n"
"v_exp_f32 v50, v50\n"
"v_exp_f32 v51, v51\n"
"v_exp_f32 v52, v52\n"
"v_exp_f32 v53, v53\n"
"v_exp_f32 v54, v54\n"
"v_exp_f32 v55, v55\n"
"buffer_load_dwordx2 v[4:5], v251, s[12:15], 0 offen sc1\n"
"s_waitcnt lgkmcnt(0)\n"
"v_mfma_f32_32x32x16_bf16 v[32:47], v[80:83], v[8:11], v[16:31]\n"
"s_setprio 2\n"
"ds_read_b128 v[80:83], v232 offset:12288\n"
"v_cvt_pk_bf16_f32 v96, v48, v49\n"
"v_cvt_pk_bf16_f32 v97, v50, v51\n"
"v_exp_f32 v56, v56\n"
"v_exp_f32 v57, v57\n"
"buffer_load_dwordx4 v[188:191], v0, s[20:23], s84 offen offset:1024\n"
"v_mfma_f32_32x32x16_bf16 v[32:47], v[84:87], v[12:15], v[32:47]\n"
"ds_read_b128 v[84:87], v232 offset:13312\n"
"v_cvt_pk_bf16_f32 v98, v52, v53\n"
"v_cvt_pk_bf16_f32 v99, v54, v55\n"
"v_exp_f32 v58, v58\n"
"v_exp_f32 v59, v59\n"
"v_mfma_f32_32x32x16_bf16 v[64:79], v[92:95], v[100:103], v[64:79]\n"
"ds_read_b128 v[92:95], v232 offset:15360\n"
"v_exp_f32 v60, v60\n"
"v_exp_f32 v61, v61\n"
"v_cvt_pk_bf16_f32 v100, v56, v57\n"
"v_cvt_pk_bf16_f32 v101, v58, v59\n"
"v_mfma_f32_32x32x16_bf16 v[64:79], v[88:91], v[96:99], v[64:79]\n"
"ds_read_b128 v[88:91], v232 offset:14336\n"
"v_exp_f32 v62, v62\n"
"v_exp_f32 v63, v63\n"
"v_cvt_pk_bf16_f32 v102, v60, v61\n"
"v_cvt_pk_bf16_f32 v103, v62, v63\n"
"s_setprio 0\n"
"v_exp_f32 v32, v32\n"
"v_exp_f32 v33, v33\n"
"v_exp_f32 v34, v34\n"
"v_exp_f32 v35, v35\n"
"v_exp_f32 v36, v36\n"
"v_exp_f32 v37, v37\n"
"v_exp_f32 v38, v38\n"
"v_exp_f32 v39, v39\n"
"s_waitcnt vmcnt(4) lgkmcnt(0)\n"
"s_barrier\n"
"v_mfma_f32_32x32x16_bf16 v[48:63], v[80:83], v[8:11], v[16:31]\n"
"s_setprio 2\n"
"ds_read_b128 v[80:83], v232 offset:16384\n"
"v_cvt_pk_bf16_f32 v96, v32, v33\n"
"v_cvt_pk_bf16_f32 v97, v34, v35\n"
"v_exp_f32 v40, v40\n"
"v_exp_f32 v41, v41\n"
"s_mov_b32 m0, s87\n"
"s_nop 0\n"
"buffer_load_dwordx4 v253, s[24:27], s82 offen lds\n"
"s_add_u32 m0, s65, 79872\n"
"v_mfma_f32_32x32x16_bf16 v[48:63], v[84:87], v[12:15], v[48:63]\n"
"ds_read_b128 v[84:87], v232 offset:17408\n"
"v_cvt_pk_bf16_f32 v98, v36, v37\n"
"v_cvt_pk_bf16_f32 v99, v38, v39\n"
"v_exp_f32 v42, v42\n"
"v_exp_f32 v43, v43\n"
"v_mfma_f32_32x32x16_bf16 v[64:79], v[92:95], v[100:103], v[64:79]\n"
"ds_read_b128 v[92:95], v232 offset:19456\n"
"v_exp_f32 v44, v44\n"
"v_exp_f32 v45, v45\n"
"v_cvt_pk_bf16_f32 v100, v40, v41\n"
"v_cvt_pk_bf16_f32 v101, v42, v43\n"
"buffer_load_dwordx4 v240, s[24:27], s82 offen lds\n"
"s_add_u32 s82, s82, 50176\n"
"v_mfma_f32_32x32x16_bf16 v[64:79], v[88:91], v[96:99], v[64:79]\n"
"ds_read_b128 v[88:91], v232 offset:18432\n"
"v_exp_f32 v46, v46\n"
"v_exp_f32 v47, v47\n"
"v_cvt_pk_bf16_f32 v102, v44, v45\n"
"v_cvt_pk_bf16_f32 v103, v46, v47\n"
"s_waitcnt vmcnt(3)\n"
"v_cmp_ne_u64 vcc, s[66:67], v[4:5]\n"
"s_setprio 0\n"
"v_exp_f32 v48, v48\n"
"v_exp_f32 v49, v49\n"
"v_exp_f32 v50, v50\n"
"v_exp_f32 v51, v51\n"
"v_exp_f32 v52, v52\n"
"v_exp_f32 v53, v53\n"
"v_exp_f32 v54, v54\n"
"v_exp_f32 v55, v55\n"
"s_cbranch_vccz L_ftp_38_%=\n"
"L_ftpr_39_%=:\n"
"s_cmp_eq_u32 s85, 0\n"
"s_cbranch_scc1 L_ftp_38_%=\n"
"s_sub_u32 s85, s85, 1\n"
"s_sleep 1\n"
"buffer_load_dwordx2 v[4:5], v251, s[12:15], 0 offen sc1\n"
"s_waitcnt vmcnt(0)\n"
"v_cmp_ne_u64 vcc, s[66:67], v[4:5]\n"
"s_nop 4\n"
"s_cbranch_vccnz L_ftpr_39_%=\n"
"L_ftp_38_%=:\n"
"s_add_u32 m0, s65, 119808\n"
"s_mov_b32 s71, 0\n"
"buffer_load_dwordx4 v248, s[12:15], s71 offen sc1 lds\n"
"s_waitcnt lgkmcnt(0)\n"
"v_mfma_f32_32x32x16_bf16 v[32:47], v[80:83], v[8:11], v[16:31]\n"
"s_setprio 2\n"
"ds_read_b128 v[80:83], v232 offset:20480\n"
"v_cvt_pk_bf16_f32 v96, v48, v49\n"
"v_cvt_pk_bf16_f32 v97, v50, v51\n"
"v_exp_f32 v56, v56\n"
"v_exp_f32 v57, v57\n"
"s_add_u32 m0, s65, 128000\n"
"s_mov_b32 s71, 8192\n"
"buffer_load_dwordx4 v248, s[12:15], s71 offen sc1 lds\n"
"v_mfma_f32_32x32x16_bf16 v[32:47], v[84:87], v[12:15], v[32:47]\n"
"ds_read_b128 v[84:87], v232 offset:21504\n"
"v_cvt_pk_bf16_f32 v98, v52, v53\n"
"v_cvt_pk_bf16_f32 v99, v54, v55\n"
"v_exp_f32 v58, v58\n"
"v_exp_f32 v59, v59\n"
"s_add_u32 m0, s65, 136192\n"
"s_mov_b32 s71, 16384\n"
"buffer_load_dwordx4 v248, s[12:15], s71 offen sc1 lds\n"
"v_mfma_f32_32x32x16_bf16 v[64:79], v[92:95], v[100:103], v[64:79]\n"
"ds_read_b128 v[92:95], v232 offset:23552\n"
"v_exp_f32 v60, v60\n"
"v_exp_f32 v61, v61\n"
"v_cvt_pk_bf16_f32 v100, v56, v57\n"
"v_cvt_pk_bf16_f32 v101, v58, v59\n"
"v_mfma_f32_32x32x16_bf16 v[64:79], v[88:91], v[96:99], v[64:79]\n"
"ds_read_b128 v[88:91], v232 offset:22528\n"
"v_exp_f32 v62, v62\n"
"v_exp_f32 v63, v63\n"
"v_cvt_pk_bf16_f32 v102, v60, v61\n"
"v_cvt_pk_bf16_f32 v103, v62, v63\n"
"s_add_u32 m0, s65, 144384\n"
"s_mov_b32 s71, 24576\n"
"buffer_load_dwordx4 v248, s[12:15], s71 offen sc1 lds\n"
"s_setprio 0\n"
"v_exp_f32 v32, v32\n"
"v_exp_f32 v33, v33\n"
"v_exp_f32 v34, v34\n"
"v_exp_f32 v35, v35\n"
"v_exp_f32 v36, v36\n"
"v_exp_f32 v37, v37\n"
"v_exp_f32 v38, v38\n"
"v_exp_f32 v39, v39\n"
"s_waitcnt lgkmcnt(0)\n"
"v_mfma_f32_32x32x16_bf16 v[48:63], v[80:83], v[8:11], v[16:31]\n"
"s_setprio 2\n"
"v_cvt_pk_bf16_f32 v96, v32, v33\n"
"v_cvt_pk_bf16_f32 v97, v34, v35\n"
"v_exp_f32 v40, v40\n"
"v_exp_f32 v41, v41\n"
"ds_read_b64_tr_b16 v[144:145], v228 offset:28672\n"
"buffer_load_dwordx4 v[192:195], v0, s[20:23], s84 offen offset:2048\n"
"v_mfma_f32_32x32x16_bf16 v[48:63], v[84:87], v[12:15], v[48:63]\n"
"v_cvt_pk_bf16_f32 v98, v36, v37\n"
"v_cvt_pk_bf16_f32 v99, v38, v39\n"
"v_exp_f32 v42, v42\n"
"v_exp_f32 v43, v43\n"
"ds_read_b64_tr_b16 v[146:147], v229 offset:28672\n"
"v_mfma_f32_32x32x16_bf16 v[64:79], v[92:95], v[100:103], v[64:79]\n"
"ds_read_b128 v[92:95], v232 offset:27648\n"
"v_exp_f32 v44, v44\n"
"v_exp_f32 v45, v45\n"
"v_cvt_pk_bf16_f32 v100, v40, v41\n"
"v_cvt_pk_bf16_f32 v101, v42, v43\n"
"ds_read_b64_tr_b16 v[148:149], v230 offset:28672\n"
"buffer_load_dwordx4 v[196:199], v0, s[20:23], s84 offen offset:3072\n"
"v_mfma_f32_32x32x16_bf16 v[64:79], v[88:91], v[96:99], v[64:79]\n"
"ds_read_b128 v[88:91], v232 offset:26624\n"
"v_exp_f32 v46, v46\n"
"v_exp_f32 v47, v47\n"
"v_cvt_pk_bf16_f32 v102, v44, v45\n"
"v_cvt_pk_bf16_f32 v103, v46, v47\n"
"ds_read_b64_tr_b16 v[150:151], v231 offset:28672\n"
"s_setprio 0\n"
"v_exp_f32 v48, v48\n"
"v_exp_f32 v49, v49\n"
"v_exp_f32 v50, v50\n"
"v_exp_f32 v51, v51\n"
"v_exp_f32 v52, v52\n"
"v_exp_f32 v53, v53\n"
"v_exp_f32 v54, v54\n"
"v_exp_f32 v55, v55\n"
"s_waitcnt lgkmcnt(0)\n"
"s_setprio 2\n"
"v_cvt_pk_bf16_f32 v96, v48, v49\n"
"v_cvt_pk_bf16_f32 v97, v50, v51\n"
"v_exp_f32 v56, v56\n"
"v_exp_f32 v57, v57\n"
"v_cvt_pk_bf16_f32 v98, v52, v53\n"
"v_cvt_pk_bf16_f32 v99, v54, v55\n"
"v_exp_f32 v58, v58\n"
"v_exp_f32 v59, v59\n"
"v_mfma_f32_32x32x16_bf16 v[64:79], v[92:95], v[100:103], v[64:79]\n"
"ds_read_b128 v[92:95], v232 offset:31744\n"
"v_exp_f32 v60, v60\n"
"v_exp_f32 v61, v61\n"
"v_cvt_pk_bf16_f32 v100, v56, v57\n"
"v_cvt_pk_bf16_f32 v101, v58, v59\n"
"v_mfma_f32_32x32x16_bf16 v[64:79], v[88:91], v[96:99], v[64:79]\n"
"v_exp_f32 v62, v62\n"
"v_exp_f32 v63, v63\n"
"v_cvt_pk_bf16_f32 v102, v60, v61\n"
"v_cvt_pk_bf16_f32 v103, v62, v63\n"
"v_bfe_u32 v4, v1, 2, 2\n"
"v_lshrrev_b32 v5, 1, v4\n"
"v_xor_b32 v5, s45, v5\n"
"v_lshlrev_b32 v5, 7, v5\n"
"v_bfe_u32 v6, v1, 4, 1\n"
"v_lshl_or_b32 v5, v6, 6, v5\n"
"v_and_b32 v6, 3, v1\n"
"v_lshl_or_b32 v5, v6, 4, v5\n"
"v_and_b32 v6, 1, v4\n"
"v_lshl_or_b32 v5, v6, 3, v5\n"
"v_lshl_add_u32 v4, v3, 3, v4\n"
"v_lshl_add_u32 v5, v4, 10, v5\n"
"v_add_u32 v249, 119808, v5\n"
"s_setprio 0\n"
"s_waitcnt lgkmcnt(0)\n"
"v_mfma_f32_32x32x16_bf16 v[64:79], v[92:95], v[100:103], v[64:79]\n"
"s_lshl_b32 s70, s45, 11\n"
"s_lshl_b32 s71, s45, 8\n"
"s_add_u32 s71, s71, 108544\n"
"v_add_u32 v56, s70, v0\n"
"v_xor_b32 v57, 8192, v56\n"
"v_lshl_add_u32 v58, v2, 3, s71\n"
"v_xor_b32 v59, 1024, v58\n"
"v_xor_b32 v60, 8, v249\n"
"v_add_u32 v61, 152576, v0\n"
"v_lshlrev_b32 v62, 16, v3\n"
"v_sub_u32 v62, v62, v3\n"
"v_not_b32 v62, v62\n"
"s_mov_b32 s70, 0x4e800000\n"
"v_cmp_nge_f32 vcc, s70, v76\n"
"s_nop 1\n"
"s_cmp_eq_u32 vcc_hi, 0\n"
"s_cbranch_scc1 L_smok_40_%=\n"
"s_lshl_b32 s70, s47, 5\n"
"s_mul_i32 s70, s70, 58368\n"
"v_subrev_u32 v4, 116736, v233\n"
"v_subrev_u32 v5, 56320, v234\n"
"v_add_u32 v6, 3072, v235\n"
"v_mov_b32 v226, 0xff800000\n"
"s_mov_b32 s71, s70\n"
"s_mov_b32 s72, 32\n"
"L_fb1_41_%=:\n"
"buffer_load_dwordx4 v[80:83], v4, s[12:15], s71 offen sc1\n"
"buffer_load_dwordx4 v[84:87], v4, s[12:15], s71 offen offset:1024 sc1\n"
"s_waitcnt vmcnt(0)\n"
"v_mfma_f32_32x32x16_bf16 v[32:47], v[80:83], v[8:11], 0\n"
"v_mfma_f32_32x32x16_bf16 v[32:47], v[84:87], v[12:15], v[32:47]\n"
"s_nop 11\n"
"v_max3_f32 v226, v226, v32, v33\n"
"v_max3_f32 v226, v226, v34, v35\n"
"v_max3_f32 v226, v226, v36, v37\n"
"v_max3_f32 v226, v226, v38, v39\n"
"v_max3_f32 v226, v226, v40, v41\n"
"v_max3_f32 v226, v226, v42, v43\n"
"v_max3_f32 v226, v226, v44, v45\n"
"v_max3_f32 v226, v226, v46, v47\n"
"s_add_u32 s71, s71, 58368\n"
"s_sub_u32 s72, s72, 1\n"
"s_cmp_lg_u32 s72, 0\n"
"s_cbranch_scc1 L_fb1_41_%=\n"
"v_mov_b32 v7, v226\n"
"s_nop 1\n"
"v_permlane32_swap_b32 v226, v7\n"
"v_max_f32 v226, v226, v7\n"
"v_sub_f32 v16, 0, v226\n"
"v_mov_b32 v64, 0\n"
"v_sub_f32 v17, 0, v226\n"
"v_mov_b32 v65, 0\n"
"v_sub_f32 v18, 0, v226\n"
"v_mov_b32 v66, 0\n"
"v_sub_f32 v19, 0, v226\n"
"v_mov_b32 v67, 0\n"
"v_sub_f32 v20, 0, v226\n"
"v_mov_b32 v68, 0\n"
"v_sub_f32 v21, 0, v226\n"
"v_mov_b32 v69, 0\n"
"v_sub_f32 v22, 0, v226\n"
"v_mov_b32 v70, 0\n"
"v_sub_f32 v23, 0, v226\n"
"v_mov_b32 v71, 0\n"
"v_sub_f32 v24, 0, v226\n"
"v_mov_b32 v72, 0\n"
"v_sub_f32 v25, 0, v226\n"
"v_mov_b32 v73, 0\n"
"v_sub_f32 v26, 0, v226\n"
"v_mov_b32 v74, 0\n"
"v_sub_f32 v27, 0, v226\n"
"v_mov_b32 v75, 0\n"
"v_sub_f32 v28, 0, v226\n"
"v_mov_b32 v76, 0\n"
"v_sub_f32 v29, 0, v226\n"
"v_mov_b32 v77, 0\n"
"v_sub_f32 v30, 0, v226\n"
"v_mov_b32 v78, 0\n"
"v_sub_f32 v31, 0, v226\n"
"v_mov_b32 v79, 0\n"
"s_mov_b32 s71, s70\n"
"s_mov_b32 s72, 32\n"
"L_fb2_42_%=:\n"
"buffer_load_dwordx4 v[80:83], v4, s[12:15], s71 offen sc1\n"
"buffer_load_dwordx4 v[84:87], v4, s[12:15], s71 offen offset:1024 sc1\n"
"buffer_load_dwordx4 v[88:91], v5, s[12:15], s71 offen sc1\n"
"buffer_load_dwordx4 v[92:95], v6, s[12:15], s71 offen sc1\n"
"s_waitcnt vmcnt(0)\n"
"v_mfma_f32_32x32x16_bf16 v[32:47], v[80:83], v[8:11], v[16:31]\n"
"v_mfma_f32_32x32x16_bf16 v[32:47], v[84:87], v[12:15], v[32:47]\n"
"s_nop 11\n"
"v_exp_f32 v32, v32\n"
"v_exp_f32 v33, v33\n"
"v_exp_f32 v34, v34\n"
"v_exp_f32 v35, v35\n"
"v_exp_f32 v36, v36\n"
"v_exp_f32 v37, v37\n"
"v_exp_f32 v38, v38\n"
"v_exp_f32 v39, v39\n"
"v_exp_f32 v40, v40\n"
"v_exp_f32 v41, v41\n"
"v_exp_f32 v42, v42\n"
"v_exp_f32 v43, v43\n"
"v_exp_f32 v44, v44\n"
"v_exp_f32 v45, v45\n"
"v_exp_f32 v46, v46\n"
"v_exp_f32 v47, v47\n"
"v_cvt_pk_bf16_f32 v96, v32, v33\n"
"v_cvt_pk_bf16_f32 v97, v34, v35\n"
"v_cvt_pk_bf16_f32 v98, v36, v37\n"
"v_cvt_pk_bf16_f32 v99, v38, v39\n"
"v_cvt_pk_bf16_f32 v100, v40, v41\n"
"v_cvt_pk_bf16_f32 v101, v42, v43\n"
"v_cvt_pk_bf16_f32 v102, v44, v45\n"
"v_cvt_pk_bf16_f32 v103, v46, v47\n"
"v_mfma_f32_32x32x16_bf16 v[64:79], v[88:91], v[96:99], v[64:79]\n"
"s_nop 0\n"
"v_mfma_f32_32x32x16_bf16 v[64:79], v[92:95], v[100:103], v[64:79]\n"
"s_add_u32 s71, s71, 58368\n"
"s_sub_u32 s72, s72, 1\n"
"s_cmp_lg_u32 s72, 0\n"
"s_cbranch_scc1 L_fb2_42_%=\n"
"s_nop 11\n"
"L_smok_40_%=:\n"
"s_cmp_eq_u32 s47, 1\n"
"s_cbranch_scc1 L_m1_43_%=\n"
"ds_write_b128 v56, v[72:75]\n"
"ds_write_b128 v56, v[76:79] offset:1024\n"
"v_mov_b32 v227, v76\n"
"s_mov_b32 exec_lo, 0\n"
"ds_write_b64 v58, v[226:227]\n"
"s_mov_b32 exec_lo, -1\n"
"s_waitcnt vmcnt(0) lgkmcnt(0)\n"
"s_barrier\n"
"ds_read_b64 v[16:17], v58\n"
"ds_read_b64 v[18:19], v59\n"
"ds_read_b128 v[32:35], v57\n"
"ds_read_b128 v[36:39], v57 offset:1024\n"
"ds_read_b64_tr_b16 v[40:41], v249 offset:0\n"
"ds_read_b64_tr_b16 v[42:43], v249 offset:4096\n"
"ds_read_b64_tr_b16 v[44:45], v249 offset:16384\n"
"ds_read_b64_tr_b16 v[46:47], v249 offset:20480\n"
"ds_read_b64_tr_b16 v[48:49], v60 offset:0\n"
"ds_read_b64_tr_b16 v[50:51], v60 offset:4096\n"
"ds_read_b64_tr_b16 v[52:53], v60 offset:16384\n"
"ds_read_b64_tr_b16 v[54:55], v60 offset:20480\n"
"ds_read_b128 v[80:83], v61 offset:0\n"
"ds_read_b128 v[84:87], v61 offset:1024\n"
"ds_read_b128 v[96:99], v61 offset:4096\n"
"s_waitcnt lgkmcnt(13)\n"
"v_max_f32 v20, v16, v18\n"
"v_sub_f32 v16, v16, v20\n"
"v_sub_f32 v18, v18, v20\n"
"v_exp_f32 v16, v16\n"
"v_exp_f32 v18, v18\n"
"v_mul_f32 v17, v17, v16\n"
"v_fma_f32 v17, v19, v18, v17\n"
"v_rcp_f32 v17, v17\n"
"s_nop 0\n"
"v_mul_f32 v16, v16, v17\n"
"v_mul_f32 v18, v18, v17\n"
"v_pk_mul_f32 v[64:65], v[64:65], v[16:17] op_sel_hi:[1,0]\n"
"v_pk_mul_f32 v[66:67], v[66:67], v[16:17] op_sel_hi:[1,0]\n"
"v_pk_mul_f32 v[68:69], v[68:69], v[16:17] op_sel_hi:[1,0]\n"
"v_pk_mul_f32 v[70:71], v[70:71], v[16:17] op_sel_hi:[1,0]\n"
"s_waitcnt lgkmcnt(11)\n"
"v_pk_fma_f32 v[64:65], v[32:33], v[18:19], v[64:65] op_sel_hi:[1,0,1]\n"
"v_pk_fma_f32 v[66:67], v[34:35], v[18:19], v[66:67] op_sel_hi:[1,0,1]\n"
"v_pk_fma_f32 v[68:69], v[36:37], v[18:19], v[68:69] op_sel_hi:[1,0,1]\n"
"v_pk_fma_f32 v[70:71], v[38:39], v[18:19], v[70:71] op_sel_hi:[1,0,1]\n"
"v_cvt_pk_bf16_f32 v20, v64, v65\n"
"v_cvt_pk_bf16_f32 v21, v66, v67\n"
"v_cvt_pk_bf16_f32 v22, v68, v69\n"
"v_cvt_pk_bf16_f32 v23, v70, v71\n"
"v_min_u32 v20, -2, v20\n"
"v_min_u32 v21, -2, v21\n"
"v_min_u32 v22, -2, v22\n"
"v_min_u32 v23, -2, v23\n"
"buffer_store_dwordx4 v[20:23], v0, s[12:15], s89 offen sc1\n"
"s_branch L_mj_44_%=\n"
"L_m1_43_%=:\n"
"ds_write_b128 v56, v[64:67]\n"
"ds_write_b128 v56, v[68:71] offset:1024\n"
"v_mov_b32 v227, v76\n"
"s_mov_b32 exec_lo, 0\n"
"ds_write_b64 v58, v[226:227]\n"
"s_mov_b32 exec_lo, -1\n"
"s_waitcnt vmcnt(0) lgkmcnt(0)\n"
"s_barrier\n"
"ds_read_b64 v[16:17], v58\n"
"ds_read_b64 v[18:19], v59\n"
"ds_read_b128 v[32:35], v57\n"
"ds_read_b128 v[36:39], v57 offset:1024\n"
"ds_read_b64_tr_b16 v[40:41], v249 offset:0\n"
"ds_read_b64_tr_b16 v[42:43], v249 offset:4096\n"
"ds_read_b64_tr_b16 v[44:45], v249 offset:16384\n"
"ds_read_b64_tr_b16 v[46:47], v249 offset:20480\n"
"ds_read_b64_tr_b16 v[48:49], v60 offset:0\n"
"ds_read_b64_tr_b16 v[50:51], v60 offset:4096\n"
"ds_read_b64_tr_b16 v[52:53], v60 offset:16384\n"
"ds_read_b64_tr_b16 v[54:55], v60 offset:20480\n"
"ds_read_b128 v[80:83], v61 offset:0\n"
"ds_read_b128 v[84:87], v61 offset:1024\n"
"ds_read_b128 v[96:99], v61 offset:4096\n"
"s_waitcnt lgkmcnt(13)\n"
"v_max_f32 v20, v16, v18\n"
"v_sub_f32 v16, v16, v20\n"
"v_sub_f32 v18, v18, v20\n"
"v_exp_f32 v16, v16\n"
"v_exp_f32 v18, v18\n"
"v_mul_f32 v17, v17, v16\n"
"v_fma_f32 v17, v19, v18, v17\n"
"v_rcp_f32 v17, v17\n"
"s_nop 0\n"
"v_mul_f32 v16, v16, v17\n"
"v_mul_f32 v18, v18, v17\n"
"v_pk_mul_f32 v[72:73], v[72:73], v[16:17] op_sel_hi:[1,0]\n"
"v_pk_mul_f32 v[74:75], v[74:75], v[16:17] op_sel_hi:[1,0]\n"
"v_pk_mul_f32 v[76:77], v[76:77], v[16:17] op_sel_hi:[1,0]\n"
"v_pk_mul_f32 v[78:79], v[78:79], v[16:17] op_sel_hi:[1,0]\n"
"s_waitcnt lgkmcnt(11)\n"
"v_pk_fma_f32 v[72:73], v[32:33], v[18:19], v[72:73] op_sel_hi:[1,0,1]\n"
"v_pk_fma_f32 v[74:75], v[34:35], v[18:19], v[74:75] op_sel_hi:[1,0,1]\n"
"v_pk_fma_f32 v[76:77], v[36:37], v[18:19], v[76:77] op_sel_hi:[1,0,1]\n"
"v_pk_fma_f32 v[78:79], v[38:39], v[18:19], v[78:79] op_sel_hi:[1,0,1]\n"
"v_cvt_pk_bf16_f32 v20, v72, v73\n"
"v_cvt_pk_bf16_f32 v21, v74, v75\n"
"v_cvt_pk_bf16_f32 v22, v76, v77\n"
"v_cvt_pk_bf16_f32 v23, v78, v79\n"
"v_and_b32 v22, v62, v22\n"
"v_min_u32 v20, -2, v20\n"
"v_min_u32 v21, -2, v21\n"
"v_min_u32 v22, -2, v22\n"
"v_min_u32 v23, -2, v23\n"
"buffer_store_dwordx4 v[20:23], v0, s[12:15], s89 offen sc1\n"
"L_mj_44_%=:\n"
"ds_read_b128 v[100:103], v61 offset:5120\n"
"ds_read_b128 v[88:91], v61 offset:2048\n"
"ds_read_b128 v[92:95], v61 offset:3072\n"
"s_lshl_b32 s70, s45, 12\n"
"v_add_u32 v4, s70, v0\n"
"s_xor_b32 s71, s65, 4096\n"
"s_add_u32 s71, s71, 110592\n"
"v_add_u32 v7, s71, v0\n"
"s_waitcnt lgkmcnt(3)\n"
"v_mfma_f32_32x32x16_bf16 v[8:23], v[40:43], v[80:83], 0\n"
"v_and_b32 v72, 15, v1\n"
"v_bfe_u32 v73, v1, 4, 1\n"
"v_lshlrev_b32 v73, 9, v73\n"
"v_lshl_add_u32 v73, v72, 4, v73\n"
"v_lshl_add_u32 v236, v3, 10, v73\n"
"v_mfma_f32_32x32x16_bf16 v[8:23], v[44:47], v[84:87], v[8:23]\n"
"v_lshl_add_u32 v73, v3, 11, v73\n"
"s_lshl_b32 s70, s47, 10\n"
"v_add_u32 v237, s70, v73\n"
"v_mul_u32_u24 v72, 114688, v72\n"
"v_lshrrev_b32 v73, 4, v1\n"
"v_lshl_add_u32 v72, v73, 3, v72\n"
"v_mfma_f32_32x32x16_bf16 v[8:23], v[48:51], v[96:99], v[8:23]\n"
"s_add_u32 s70, s83, s46\n"
"s_lshl_b32 s70, s70, 5\n"
"s_lshl_b32 s71, s47, 10\n"
"s_add_u32 s71, s71, s61\n"
"s_mul_i32 s71, s71, 1792\n"
"s_add_u32 s70, s70, s71\n"
"s_add_u32 s70, s70, 896\n"
"v_add_u32 v239, s70, v72\n"
"s_waitcnt lgkmcnt(2)\n"
"v_mfma_f32_32x32x16_bf16 v[8:23], v[52:55], v[100:103], v[8:23]\n"
"v_mfma_f32_32x32x16_bf16 v[56:71], v[48:51], v[80:83], 0\n"
"s_or_b32 s70, s44, s45\n"
"s_cmp_lg_u32 s70, 0\n"
"s_cbranch_scc1 L_ep_45_%=\n"
"s_mov_b64 exec, 1\n"
"buffer_store_dwordx2 v[254:255], off, s[12:15], 0 sc1\n"
"s_mov_b64 exec, -1\n"
"L_ep_45_%=:\n"
"v_mfma_f32_32x32x16_bf16 v[56:71], v[52:55], v[84:87], v[56:71]\n"
"s_waitcnt lgkmcnt(0)\n"
"ds_read_b64_tr_b16 v[152:153], v228 offset:0\n"
"ds_read_b64_tr_b16 v[154:155], v229 offset:0\n"
"ds_read_b64_tr_b16 v[156:157], v230 offset:0\n"
"ds_read_b64_tr_b16 v[158:159], v231 offset:0\n"
"v_mfma_f32_32x32x16_bf16 v[56:71], v[40:43], v[88:91], v[56:71]\n"
"ds_read_b64_tr_b16 v[160:161], v228 offset:14336\n"
"ds_read_b64_tr_b16 v[162:163], v229 offset:14336\n"
"ds_read_b64_tr_b16 v[164:165], v230 offset:14336\n"
"ds_read_b64_tr_b16 v[166:167], v231 offset:14336\n"
"v_mfma_f32_32x32x16_bf16 v[56:71], v[44:47], v[92:95], v[56:71]\n"
"v_cvt_pk_bf16_f32 v24, v8, v9\n"
"v_cvt_pk_bf16_f32 v25, v10, v11\n"
"v_cvt_pk_bf16_f32 v26, v12, v13\n"
"v_cvt_pk_bf16_f32 v27, v14, v15\n"
"v_cvt_pk_bf16_f32 v28, v16, v17\n"
"v_cvt_pk_bf16_f32 v29, v18, v19\n"
"v_cvt_pk_bf16_f32 v30, v20, v21\n"
"v_cvt_pk_bf16_f32 v31, v22, v23\n"
"ds_write_b128 v4, v[24:27] offset:32768\n"
"ds_write_b128 v4, v[28:31] offset:33792\n"
"s_lshl_b32 s72, s61, 15\n"
"s_lshl_b32 s71, s45, 12\n"
"s_add_u32 s72, s72, s71\n"
"s_add_u32 s72, s72, 8060928\n"
"s_mov_b32 s73, 28672\n"
"v_cvt_pk_bf16_f32 v32, v56, v57\n"
"v_cvt_pk_bf16_f32 v33, v58, v59\n"
"v_cvt_pk_bf16_f32 v34, v60, v61\n"
"v_cvt_pk_bf16_f32 v35, v62, v63\n"
"v_cvt_pk_bf16_f32 v36, v64, v65\n"
"v_cvt_pk_bf16_f32 v37, v66, v67\n"
"v_cvt_pk_bf16_f32 v38, v68, v69\n"
"v_cvt_pk_bf16_f32 v39, v70, v71\n"
"ds_write_b128 v4, v[32:35] offset:34816\n"
"ds_write_b128 v4, v[36:39] offset:35840\n"
"s_waitcnt lgkmcnt(0)\n"
"s_barrier\n"
"ds_read_b128 v[24:27], v237 offset:32768\n"
"ds_read_b128 v[28:31], v237 offset:33024\n"
"ds_read_b128 v[32:35], v237 offset:36864\n"
"ds_read_b128 v[36:39], v237 offset:37120\n"
"ds_read_b128 v[40:43], v237 offset:40960\n"
"ds_read_b128 v[44:47], v237 offset:41216\n"
"ds_read_b128 v[48:51], v237 offset:45056\n"
"ds_read_b128 v[52:55], v237 offset:45312\n"
"s_waitcnt lgkmcnt(7)\n"
"v_mfma_f32_16x16x32_bf16 v[8:11], v[168:171], v[24:27], 0\n"
"ds_read_b128 v[24:27], v237 offset:49152\n"
"s_waitcnt lgkmcnt(7)\n"
"v_mfma_f32_16x16x32_bf16 v[12:15], v[168:171], v[28:31], 0\n"
"ds_read_b128 v[28:31], v237 offset:49408\n"
"s_waitcnt lgkmcnt(7)\n"
"v_mfma_f32_16x16x32_bf16 v[8:11], v[172:175], v[32:35], v[8:11]\n"
"ds_read_b128 v[32:35], v237 offset:53248\n"
"s_waitcnt lgkmcnt(7)\n"
"v_mfma_f32_16x16x32_bf16 v[12:15], v[172:175], v[36:39], v[12:15]\n"
"ds_read_b128 v[36:39], v237 offset:53504\n"
"s_waitcnt lgkmcnt(7)\n"
"v_mfma_f32_16x16x32_bf16 v[8:11], v[176:179], v[40:43], v[8:11]\n"
"ds_read_b128 v[40:43], v237 offset:57344\n"
"s_waitcnt lgkmcnt(7)\n"
"v_mfma_f32_16x16x32_bf16 v[12:15], v[176:179], v[44:47], v[12:15]\n"
"ds_read_b128 v[44:47], v237 offset:57600\n"
"s_waitcnt lgkmcnt(7)\n"
"v_mfma_f32_16x16x32_bf16 v[8:11], v[180:183], v[48:51], v[8:11]\n"
"ds_read_b128 v[48:51], v237 offset:61440\n"
"s_waitcnt lgkmcnt(7)\n"
"v_mfma_f32_16x16x32_bf16 v[12:15], v[180:183], v[52:55], v[12:15]\n"
"ds_read_b128 v[52:55], v237 offset:61696\n"
"s_waitcnt lgkmcnt(7)\n"
"v_mfma_f32_16x16x32_bf16 v[8:11], v[184:187], v[24:27], v[8:11]\n"
"s_waitcnt lgkmcnt(6)\n"
"v_mfma_f32_16x16x32_bf16 v[12:15], v[184:187], v[28:31], v[12:15]\n"
"buffer_load_dwordx4 v[64:67], v0, s[12:15], s72 offen offset:0 sc1\n"
"s_waitcnt lgkmcnt(5)\n"
"v_mfma_f32_16x16x32_bf16 v[8:11], v[188:191], v[32:35], v[8:11]\n"
"s_waitcnt lgkmcnt(4)\n"
"v_mfma_f32_16x16x32_bf16 v[12:15], v[188:191], v[36:39], v[12:15]\n"
"buffer_load_dwordx4 v[68:71], v0, s[12:15], s72 offen offset:1024 sc1\n"
"s_waitcnt lgkmcnt(3)\n"
"v_mfma_f32_16x16x32_bf16 v[8:11], v[192:195], v[40:43], v[8:11]\n"
"s_waitcnt lgkmcnt(2)\n"
"v_mfma_f32_16x16x32_bf16 v[12:15], v[192:195], v[44:47], v[12:15]\n"
"buffer_load_dwordx4 v[72:75], v0, s[12:15], s72 offen offset:2048 sc1\n"
"s_waitcnt lgkmcnt(1)\n"
"v_mfma_f32_16x16x32_bf16 v[8:11], v[196:199], v[48:51], v[8:11]\n"
"s_waitcnt lgkmcnt(0)\n"
"v_mfma_f32_16x16x32_bf16 v[12:15], v[196:199], v[52:55], v[12:15]\n"
"buffer_load_dwordx4 v[76:79], v0, s[12:15], s72 offen offset:3072 sc1\n"
"s_cmp_eq_u32 s47, 1\n"
"s_cbranch_scc1 L_par1_46_%=\n"
"s_nop 4\n"
"ds_write_b128 v252, v[12:15]\n"
"s_mov_b32 s71, 1.0\n"
"s_branch L_parj_47_%=\n"
"L_par1_46_%=:\n"
"s_nop 2\n"
"ds_write_b128 v252, v[8:11]\n"
"s_mov_b32 s71, -1.0\n"
"s_nop 0\n"
"v_mov_b32 v8, v12\n"
"v_mov_b32 v9, v13\n"
"v_mov_b32 v10, v14\n"
"v_mov_b32 v11, v15\n"
"L_parj_47_%=:\n"
"v_and_b32 v5, 15, v1\n"
"v_lshrrev_b32 v6, 5, v1\n"
"s_lshl_b32 s70, s45, 1\n"
"v_or_b32 v6, s70, v6\n"
"v_xor_b32 v6, v6, v5\n"
"v_lshlrev_b32 v6, 4, v6\n"
"v_lshl_add_u32 v6, v5, 8, v6\n"
"v_bfe_u32 v84, v1, 4, 1\n"
"v_lshl_add_u32 v6, v84, 3, v6\n"
"v_add_u32 v83, 119808, v6\n"
"s_lshl_b32 s70, s47, 7\n"
"v_xor_b32 v86, s70, v6\n"
"s_lshl_b32 s70, s47, 12\n"
"s_add_u32 s70, s70, 128000\n"
"v_add_u32 v86, s70, v86\n"
"v_xor_b32 v87, 128, v86\n"
"v_lshrrev_b32 v84, 4, v1\n"
"s_lshl_b32 s70, s45, 2\n"
"v_add_u32 v84, s70, v84\n"
"v_and_b32 v85, 15, v84\n"
"v_xor_b32 v85, v85, v5\n"
"v_lshlrev_b32 v85, 4, v85\n"
"v_lshl_add_u32 v85, v84, 8, v85\n"
"v_add_u32 v85, 119808, v85\n"
"v_mul_u32_u24 v88, 114688, v84\n"
"v_and_b32 v89, 7, v5\n"
"v_lshl_add_u32 v88, v89, 4, v88\n"
"v_lshrrev_b32 v89, 3, v5\n"
"v_mul_u32_u24 v89, 448, v89\n"
"v_add_u32 v88, v88, v89\n"
"v_mul_u32_u24 v84, 1792, v84\n"
"v_lshl_add_u32 v84, v5, 4, v84\n"
"s_mul_i32 s74, s61, 57344\n"
"s_lshl_b32 s70, s79, 1\n"
"s_add_u32 s74, s74, s70\n"
"s_mul_i32 s75, s61, 1792\n"
"s_lshl_b32 s70, s83, 5\n"
"s_add_u32 s75, s75, s70\n"
"s_add_u32 s75, s75, 896\n"
"s_waitcnt vmcnt(0)\n"
"L_or_48_%=:\n"
"v_max3_u32 v80, v64, v65, v66\n"
"v_max3_u32 v81, v67, v68, v69\n"
"v_max3_u32 v80, v80, v70, v71\n"
"v_max3_u32 v81, v81, v72, v73\n"
"v_max3_u32 v80, v80, v74, v75\n"
"v_max3_u32 v81, v81, v76, v77\n"
"v_max3_u32 v80, v80, v78, v79\n"
"v_max_u32 v80, v80, v81\n"
"v_cmp_eq_u32 vcc, -1, v80\n"
"ds_write_b128 v4, v[64:67] offset:0\n"
"ds_write_b128 v4, v[68:71] offset:1024\n"
"ds_write_b128 v4, v[72:75] offset:2048\n"
"ds_write_b128 v4, v[76:79] offset:3072\n"
"s_nop 0\n"
"s_cbranch_vccz L_ok_49_%=\n"
"s_cmp_eq_u32 s85, 0\n"
"s_cbranch_scc1 L_ok_49_%=\n"
"s_sub_u32 s85, s85, 1\n"
"s_waitcnt lgkmcnt(0)\n"
"buffer_load_dwordx4 v[64:67], v0, s[12:15], s72 offen offset:0 sc1\n"
"buffer_load_dwordx4 v[68:71], v0, s[12:15], s72 offen offset:1024 sc1\n"
"buffer_load_dwordx4 v[72:75], v0, s[12:15], s72 offen offset:2048 sc1\n"
"buffer_load_dwordx4 v[76:79], v0, s[12:15], s72 offen offset:3072 sc1\n"
"s_waitcnt vmcnt(0)\n"
"s_branch L_or_48_%=\n"
"L_ok_49_%=:\n"
"s_waitcnt lgkmcnt(0)\n"
"s_barrier\n"
"ds_read_b128 v[16:19], v7\n"
"ds_read_b128 v[24:27], v236 offset:0\n"
"ds_read_b128 v[28:31], v236 offset:2048\n"
"ds_read_b128 v[32:35], v236 offset:4096\n"
"ds_read_b128 v[36:39], v236 offset:6144\n"
"ds_read_b128 v[40:43], v236 offset:8192\n"
"ds_read_b128 v[44:47], v236 offset:10240\n"
"ds_read_b128 v[48:51], v236 offset:12288\n"
"ds_read_b128 v[52:55], v236 offset:14336\n"
"s_waitcnt lgkmcnt(8)\n"
"v_add_f32 v20, v8, v16\n"
"v_sub_f32 v16, v8, v16\n"
"v_add_f32 v21, v9, v17\n"
"v_sub_f32 v17, v9, v17\n"
"v_add_f32 v22, v10, v18\n"
"v_sub_f32 v18, v10, v18\n"
"v_add_f32 v23, v11, v19\n"
"v_sub_f32 v19, v11, v19\n"
"v_mul_f32 v16, s71, v16\n"
"v_mul_f32 v17, s71, v17\n"
"v_mul_f32 v18, s71, v18\n"
"v_mul_f32 v19, s71, v19\n"
"v_cvt_pk_bf16_f32 v20, v20, v21\n"
"v_cvt_pk_bf16_f32 v21, v22, v23\n"
"v_cvt_pk_bf16_f32 v16, v16, v17\n"
"v_cvt_pk_bf16_f32 v17, v18, v19\n"
"ds_write_b64 v86, v[20:21]\n"
"ds_write_b64 v87, v[16:17]\n"
"s_waitcnt lgkmcnt(9)\n"
"v_mfma_f32_16x16x32_bf16 v[56:59], v[104:107], v[24:27], 0\n"
"ds_read_b128 v[24:27], v236 offset:16384\n"
"s_waitcnt lgkmcnt(9)\n"
"v_mfma_f32_16x16x32_bf16 v[56:59], v[108:111], v[28:31], v[56:59]\n"
"ds_read_b128 v[28:31], v236 offset:18432\n"
"s_waitcnt lgkmcnt(2)\n"
"s_barrier\n"
"ds_read_b128 v[8:11], v85 offset:8192\n"
"v_mfma_f32_16x16x32_bf16 v[56:59], v[112:115], v[32:35], v[56:59]\n"
"ds_read_b128 v[32:35], v236 offset:20480\n"
"v_mfma_f32_16x16x32_bf16 v[56:59], v[116:119], v[36:39], v[56:59]\n"
"ds_read_b128 v[36:39], v236 offset:22528\n"
"v_mfma_f32_16x16x32_bf16 v[56:59], v[120:123], v[40:43], v[56:59]\n"
"ds_read_b128 v[40:43], v236 offset:24576\n"
"s_waitcnt lgkmcnt(3)\n"
"buffer_store_dwordx4 v[8:11], v88, s[28:31], s75 offen sc1\n"
"v_mfma_f32_16x16x32_bf16 v[56:59], v[124:127], v[44:47], v[56:59]\n"
"ds_read_b128 v[44:47], v236 offset:26624\n"
"v_mfma_f32_16x16x32_bf16 v[56:59], v[128:131], v[48:51], v[56:59]\n"
"ds_read_b128 v[48:51], v236 offset:28672\n"
"v_mfma_f32_16x16x32_bf16 v[56:59], v[132:135], v[52:55], v[56:59]\n"
"ds_read_b128 v[52:55], v236 offset:30720\n"
"v_mfma_f32_16x16x32_bf16 v[56:59], v[136:139], v[24:27], v[56:59]\n"
"ds_read_b128 v[24:27], v236 offset:256\n"
"v_mfma_f32_16x16x32_bf16 v[56:59], v[140:143], v[28:31], v[56:59]\n"
"ds_read_b128 v[28:31], v236 offset:2304\n"
"s_waitcnt lgkmcnt(7)\n"
"v_mfma_f32_16x16x32_bf16 v[56:59], v[144:147], v[32:35], v[56:59]\n"
"ds_read_b128 v[32:35], v236 offset:4352\n"
"s_waitcnt lgkmcnt(7)\n"
"v_mfma_f32_16x16x32_bf16 v[56:59], v[148:151], v[36:39], v[56:59]\n"
"ds_read_b128 v[36:39], v236 offset:6400\n"
"s_waitcnt lgkmcnt(7)\n"
"v_mfma_f32_16x16x32_bf16 v[56:59], v[152:155], v[40:43], v[56:59]\n"
"ds_read_b128 v[40:43], v236 offset:8448\n"
"s_waitcnt lgkmcnt(7)\n"
"v_mfma_f32_16x16x32_bf16 v[56:59], v[156:159], v[44:47], v[56:59]\n"
"ds_read_b128 v[44:47], v236 offset:10496\n"
"s_waitcnt lgkmcnt(7)\n"
"v_mfma_f32_16x16x32_bf16 v[56:59], v[160:163], v[48:51], v[56:59]\n"
"ds_read_b128 v[48:51], v236 offset:12544\n"
"s_waitcnt lgkmcnt(7)\n"
"v_mfma_f32_16x16x32_bf16 v[56:59], v[164:167], v[52:55], v[56:59]\n"
"ds_read_b128 v[52:55], v236 offset:14592\n"
"s_waitcnt lgkmcnt(7)\n"
"v_mfma_f32_16x16x32_bf16 v[60:63], v[104:107], v[24:27], 0\n"
"ds_read_b128 v[24:27], v236 offset:16640\n"
"s_waitcnt lgkmcnt(7)\n"
"v_mfma_f32_16x16x32_bf16 v[60:63], v[108:111], v[28:31], v[60:63]\n"
"ds_read_b128 v[28:31], v236 offset:18688\n"
"s_waitcnt lgkmcnt(7)\n"
"v_mfma_f32_16x16x32_bf16 v[60:63], v[112:115], v[32:35], v[60:63]\n"
"ds_read_b128 v[32:35], v236 offset:20736\n"
"s_waitcnt lgkmcnt(7)\n"
"v_mfma_f32_16x16x32_bf16 v[60:63], v[116:119], v[36:39], v[60:63]\n"
"ds_read_b128 v[36:39], v236 offset:22784\n"
"v_cvt_pk_bf16_f32 v56, v56, v57\n"
"v_cvt_pk_bf16_f32 v57, v58, v59\n"
"ds_write_b64 v83, v[56:57] offset:0\n"
"s_waitcnt lgkmcnt(8)\n"
"v_mfma_f32_16x16x32_bf16 v[60:63], v[120:123], v[40:43], v[60:63]\n"
"ds_read_b128 v[40:43], v236 offset:24832\n"
"s_waitcnt lgkmcnt(8)\n"
"v_mfma_f32_16x16x32_bf16 v[60:63], v[124:127], v[44:47], v[60:63]\n"
"ds_read_b128 v[44:47], v236 offset:26880\n"
"s_waitcnt lgkmcnt(8)\n"
"v_mfma_f32_16x16x32_bf16 v[60:63], v[128:131], v[48:51], v[60:63]\n"
"ds_read_b128 v[48:51], v236 offset:28928\n"
"s_waitcnt lgkmcnt(8)\n"
"v_mfma_f32_16x16x32_bf16 v[60:63], v[132:135], v[52:55], v[60:63]\n"
"ds_read_b128 v[52:55], v236 offset:30976\n"
"s_waitcnt lgkmcnt(8)\n"
"v_mfma_f32_16x16x32_bf16 v[60:63], v[136:139], v[24:27], v[60:63]\n"
"s_waitcnt lgkmcnt(7)\n"
"v_mfma_f32_16x16x32_bf16 v[60:63], v[140:143], v[28:31], v[60:63]\n"
"s_waitcnt lgkmcnt(6)\n"
"v_mfma_f32_16x16x32_bf16 v[60:63], v[144:147], v[32:35], v[60:63]\n"
"s_waitcnt lgkmcnt(5)\n"
"v_mfma_f32_16x16x32_bf16 v[60:63], v[148:151], v[36:39], v[60:63]\n"
"s_waitcnt lgkmcnt(3)\n"
"v_mfma_f32_16x16x32_bf16 v[60:63], v[152:155], v[40:43], v[60:63]\n"
"s_waitcnt lgkmcnt(2)\n"
"v_mfma_f32_16x16x32_bf16 v[60:63], v[156:159], v[44:47], v[60:63]\n"
"s_waitcnt lgkmcnt(1)\n"
"v_mfma_f32_16x16x32_bf16 v[60:63], v[160:163], v[48:51], v[60:63]\n"
"s_waitcnt lgkmcnt(0)\n"
"v_mfma_f32_16x16x32_bf16 v[60:63], v[164:167], v[52:55], v[60:63]\n"
"s_nop 7\n"
"v_cvt_pk_bf16_f32 v60, v60, v61\n"
"v_cvt_pk_bf16_f32 v61, v62, v63\n"
"ds_write_b64 v83, v[60:61] offset:4096\n"
"s_waitcnt lgkmcnt(0)\n"
"s_barrier\n"
"ds_read_b128 v[12:15], v85\n"
"s_waitcnt lgkmcnt(0)\n"
"buffer_store_dwordx4 v[12:15], v84, s[28:31], s74 offen\n"
"L_end_%=:\n"
    : "+{v0}"(tid), "+{s[0:1]}"(args), "+{s2}"(blk)
    : "{s[20:21]}"(TAB_PTR)
    : "v1","v2","v3","v4","v5","v6","v7","v8","v9","v10","v11","v12","v13","v14","v15","v16","v17","v18","v19","v20","v21","v22","v23","v24","v25","v26","v27","v28","v29","v30","v31","v32","v33","v34","v35","v36","v37","v38","v39","v40","v41","v42","v43","v44","v45","v46","v47","v48","v49","v50","v51","v52","v53","v54","v55","v56","v57","v58","v59","v60","v61","v62","v63","v64","v65","v66","v67","v68","v69","v70","v71","v72","v73","v74","v75","v76","v77","v78","v79","v80","v81","v82","v83","v84","v85","v86","v87","v88","v89","v90","v91","v92","v93","v94","v95","v96","v97","v98","v99","v100","v101","v102","v103","v104","v105","v106","v107","v108","v109","v110","v111","v112","v113","v114","v115","v116","v117","v118","v119","v120","v121","v122","v123","v124","v125","v126","v127","v128","v129","v130","v131","v132","v133","v134","v135","v136","v137","v138","v139","v140","v141","v142","v143","v144","v145","v146","v147","v148","v149","v150","v151","v152","v153","v154","v155","v156","v157","v158","v159","v160","v161","v162","v163","v164","v165","v166","v167","v168","v169","v170","v171","v172","v173","v174","v175","v176","v177","v178","v179","v180","v181","v182","v183","v184","v185","v186","v187","v188","v189","v190","v191","v192","v193","v194","v195","v196","v197","v198","v199","v200","v201","v202","v203","v204","v205","v206","v207","v208","v209","v210","v211","v212","v213","v214","v215","v216","v217","v218","v219","v220","v221","v222","v223","v224","v225","v226","v227","v228","v229","v230","v231","v232","v233","v234","v235","v236","v237","v238","v239","v240","v241","v242","v243","v244","v245","v246","v247","v248","v249","v250","v251","v252","v253","v254","v255","s3","s4","s5","s6","s7","s8","s9","s10","s11","s12","s13","s14","s15","s16","s17","s18","s19","s22","s23","s24","s25","s26","s27","s28","s29","s30","s31","s34","s35","s36","s37","s38","s39","s40","s41","s42","s43","s44","s45","s46","s47","s48","s49","s50","s51","s52","s53","s54","s55","s56","s57","s58","s59","s60","s61","s62","s63","s64","s65","s66","s67","s68","s69","s70","s71","s72","s73","s74","s75","s76","s77","s78","s79","s80","s81","s82","s83","s84","s85","s86","s87","s88","s89","s90","s91","s92","s93","s94","s95","s96","s97","s98","s99","vcc","scc","memory");
}

extern "C" void kernel_launch(void* const* d_in, const int* in_sizes, int n_in, void* d_out, int out_size, void* d_ws, size_t ws_size,
                              hipStream_t stream) {
    (void)in_sizes; (void)n_in; (void)out_size; (void)ws_size;
    (void)hipFuncSetAttribute((const void*)concat_heads__softmax_full_mha__spectral_gfnet__prompt_kernel,
                              hipFuncAttributeMaxDynamicSharedMemorySize, 159744);
    concat_heads__softmax_full_mha__spectral_gfnet__prompt_kernel<<<256, 512, 159744, stream>>>(
        d_in[0], d_in[1], d_in[2], d_in[3], d_in[4], d_in[5], d_out, d_ws);
}
```
